```python
import torch

def _softmax(x, axis=-1):
    return torch.softmax(x, dim=axis)

def _gelu(x):
    return torch.nn.functional.gelu(x, approximate='tanh')

def _silu(x):
    return torch.nn.functional.silu(x)

def _sigmoid(x):
    return torch.sigmoid(x)

def _layernorm(x, axis=-1, eps=1e-05):
    m = x.mean(dim=axis, keepdim=True)
    v = x.var(dim=axis, keepdim=True, unbiased=False)
    return (x - m) / torch.sqrt(v + eps)

def _scatter_add(tgt, idx, src, axis=0):
    return tgt.clone().index_add_(axis, idx, src)

def _segment_sum(src, seg, n):
    out = torch.zeros((n,) + src.shape[1:], dtype=src.dtype)
    return out.index_add_(0, seg, src)

def _pad(x, pw):
    flat = tuple((v for pair in reversed(pw) for v in pair))
    return torch.nn.functional.pad(x, flat)
kernel_name = 'gather_scatter__molecular_angular_painn__graph'

def setup_inputs():

    _w = lambda a: (lambda t: t.to(torch.bfloat16) if t.is_floating_point() else t)(torch.as_tensor(a))
    N, deg, D = (25000, 32, 64)
    n_rbf = max(16, 4)
    E = N * deg
    cutoff = 5.0
    freqs = (torch.arange(1, n_rbf + 1) * torch.pi / cutoff).to(float)
    return {'sv': _w((lambda *s: torch.randn(*s, dtype=torch.bfloat16) * 0.4)(N, 4, D) * 0.3), 'pos': _w((lambda *s: torch.randn(*s, dtype=torch.bfloat16) * 1.6)(N, 3) * 1.2), 'src': _w((lambda lo, hi, *s: torch.randint(lo, hi, s))(0, N, E).to(torch.int64)), 'dst': _w((lambda lo, hi, *s: torch.randint(lo, hi, s))(0, N, E).to(torch.int64)), 'freqs': _w(freqs), 'w_phi1': _w((lambda *s: torch.randn(*s, dtype=torch.bfloat16) * 3.2)(D, D) / D ** 0.5), 'w_phi2': _w((lambda *s: torch.randn(*s, dtype=torch.bfloat16) * 1.6)(D, 3 * D) / D ** 0.5), 'w_rbf': _w((lambda *s: torch.randn(*s, dtype=torch.bfloat16) * 1.6)(n_rbf, 3 * D) / n_rbf ** 0.5)}

def reference(sv, pos, src, dst, freqs, w_phi1, w_phi2, w_rbf):
    N, _, D = sv.shape
    E = src.shape[0]
    PI, cutoff = (3.141592653589793, 5.0)
    s, v = (sv[:, 0, :].clone(), sv[:, 1:, :].clone())
    r_vec = pos[src] - pos[dst]
    d = torch.sqrt((r_vec * r_vec).sum(-1) + 1e-08)
    r_hat = r_vec / d[:, None]
    rbf = torch.sin(d[:, None] * freqs[None, :]) / d[:, None]
    fcut = 0.5 * (torch.cos(PI * d / cutoff) + 1.0)
    fcut = torch.where(d < cutoff, fcut, 0.0 * d)
    Wf = rbf @ w_rbf * fcut[:, None]
    phi = (_silu(s @ w_phi1) @ w_phi2)[src]
    m = phi * Wf
    m_s, m_vv, m_vr = (m[:, :D].clone(), m[:, D:2 * D].clone(), m[:, 2 * D:].clone())
    ds = _segment_sum(m_s, dst, N)
    dv_e = m_vv[:, None, :] * v[src] + m_vr[:, None, :] * r_hat[:, :, None]
    dv = _segment_sum(dv_e.reshape(E, 3 * D), dst, N).reshape(N, 3, D)
    return sv + torch.concatenate([ds[:, None, :], dv], axis=1)
```

```cpp
#include <hip/hip_runtime.h>
#include <hip/hip_bf16.h>
#include <stdint.h>
#define MIMIC 0
typedef unsigned short u16;
__device__ __forceinline__ float bf2f(u16 x){ return __uint_as_float(((unsigned)x)<<16); }
__device__ __forceinline__ float rb(float x){
  unsigned u = __float_as_uint(x); u = (u + 0x7FFF + ((u>>16)&1)) & 0xFFFF0000u; return __uint_as_float(u); }
__device__ __forceinline__ u16 f2bf(float x){ unsigned u = __float_as_uint(x); return (u16)((u + 0x7FFF + ((u>>16)&1))>>16); }
__device__ __forceinline__ float g1(float x){ return MIMIC>=1 ? rb(x) : x; }
__device__ __forceinline__ float g2(float x){ return MIMIC>=2 ? rb(x) : x; }

__global__ void k_phi(const u16* sv, const u16* w1, const u16* w2, float* phi, int N){
  int n = blockIdx.x; int t = threadIdx.x;
  __shared__ float s[64], h[64];
  if (t<64) s[t] = bf2f(sv[(size_t)n*256+t]);
  __syncthreads();
  if (t<64){ float a=0; for(int j=0;j<64;j++) a += s[j]*bf2f(w1[j*64+t]); a = g2(a); h[t] = g2(a/(1.f+expf(-a))); }
  __syncthreads();
  float a=0; for(int j=0;j<64;j++) a += h[j]*bf2f(w2[j*192+t]);
  phi[(size_t)n*192+t] = g2(a);
}
__global__ void k_edge(const u16* sv, const u16* pos, const int* src, const int* dst, const u16* freqs, const u16* wr, const float* phi, float* acc, int E){
  int e = blockIdx.x*4 + (threadIdx.x>>6); int c = threadIdx.x&63;
  if (e>=E) return;
  int s = src[e], d_ = dst[e];
  float r[3]; float ss=0;
  for(int k=0;k<3;k++){ r[k] = g1(bf2f(pos[s*3+k]) - bf2f(pos[d_*3+k])); ss += g1(r[k]*r[k]); }
  float d = g1(sqrtf(g1(g1(ss)+1e-8f)));
  float rh[3]; for(int k=0;k<3;k++) rh[k] = g1(r[k]/d);
  float fc = g2(0.5f*g2(g2(cosf(g1(g1(3.141592653589793f*d)/5.0f)))+1.0f));
  if (!(d<5.0f)) fc = 0.f;
  float w[3]={0,0,0};
  for(int q=0;q<16;q++){ float rbf = g2(g2(sinf(g1(d*bf2f(freqs[q]))))/d);
    for(int j=0;j<3;j++) w[j] += rbf*bf2f(wr[q*192+j*64+c]); }
  float m[3]; for(int j=0;j<3;j++) m[j] = g2(phi[(size_t)s*192+j*64+c]*g2(g2(w[j])*fc));
  atomicAdd(&acc[(size_t)d_*256+c], m[0]);
  for(int k=0;k<3;k++) atomicAdd(&acc[(size_t)d_*256+64+k*64+c], m[1]*bf2f(sv[(size_t)s*256+64+k*64+c]) + m[2]*rh[k]);
}
__global__ void k_fin(const u16* sv, const float* acc, u16* out, int n){
  int i = blockIdx.x*256+threadIdx.x; if (i<n) out[i] = f2bf(bf2f(sv[i])+acc[i]);
}
extern "C" void kernel_launch(void* const* d_in, const int* in_sizes, int n_in, void* d_out, int out_size, void* d_ws, size_t ws_size, hipStream_t stream){
  const u16* sv=(const u16*)d_in[0]; const u16* pos=(const u16*)d_in[1]; const int* src=(const int*)d_in[2]; const int* dst=(const int*)d_in[3];
  const u16* freqs=(const u16*)d_in[4]; const u16* w1=(const u16*)d_in[5]; const u16* w2=(const u16*)d_in[6]; const u16* wr=(const u16*)d_in[7];
  int N = in_sizes[0]/256; int E = in_sizes[2];
  float* phi=(float*)d_ws; float* acc = phi + (size_t)N*192;
  hipMemsetAsync(acc, 0, (size_t)N*256*4, stream);
  k_phi<<<N,192,0,stream>>>(sv,w1,w2,phi,N);
  k_edge<<<(E+3)/4,256,0,stream>>>(sv,pos,src,dst,freqs,wr,phi,acc,E);
  k_fin<<<(N*256+255)/256,256,0,stream>>>(sv,acc,(u16*)d_out,N*256);
}
```

```cpp
#include <hip/hip_runtime.h>
#include <stdint.h>
typedef unsigned u32;
typedef unsigned short u16;
typedef __attribute__((ext_vector_type(2))) float f32x2;
typedef __attribute__((ext_vector_type(16))) float f32x16;
typedef __attribute__((ext_vector_type(2))) __bf16 bf16x2;
typedef __attribute__((ext_vector_type(8))) __bf16 bf16x8;
typedef __attribute__((ext_vector_type(2))) _Float16 f16x2;
typedef __attribute__((ext_vector_type(4))) short s16x4;
typedef __attribute__((ext_vector_type(2))) unsigned u32x2;
typedef __attribute__((ext_vector_type(3))) unsigned u32x3;
typedef __attribute__((ext_vector_type(4))) unsigned u32x4;
typedef __attribute__((ext_vector_type(4))) int i32x4;
#define DI __device__ __forceinline__
#define GAS __attribute__((address_space(1)))
#define LAS __attribute__((address_space(3)))

DI u32 pk2(f32x2 v){ bf16x2 r=__builtin_convertvector(v,bf16x2); return __builtin_bit_cast(u32,r); }
DI u32 pk2(float a, float b){ f32x2 v={a,b}; return pk2(v); }
DI u32 pkh(float a, float b){ f32x2 v={a,b}; f16x2 r=__builtin_convertvector(v,f16x2); return __builtin_bit_cast(u32,r); }
DI float asf(u32 x){ return __builtin_bit_cast(float,x); }
DI u32 asu(float x){ return __builtin_bit_cast(u32,x); }
DI float bf2f(u32 x){ return asf(x<<16); }
DI float rbf(float x){ return asf(pk2(x,0.f)<<16); }
DI f32x16 mfma32(u32x4 a, u32x4 b, f32x16 c){ return __builtin_amdgcn_mfma_f32_32x32x16_bf16(__builtin_bit_cast(bf16x8,a),__builtin_bit_cast(bf16x8,b),c,0,0,0); }
DI void dma16(const void* g, char* l){ __builtin_amdgcn_global_load_lds((const GAS u32*)g,(LAS u32*)l,16,0,0); }
DI u32x2 trread(const char* p){ s16x4 r = __builtin_amdgcn_ds_read_tr16_b64_v4i16((LAS s16x4*)p); return __builtin_bit_cast(u32x2,r); }
#define WAITVM(n) asm volatile("s_waitcnt vmcnt(" #n ")" ::: "memory")
#define WAITLG0() asm volatile("s_waitcnt lgkmcnt(0)" ::: "memory")
#define PIN(x) asm volatile("" : "+v"(x))
DI u32 ldsaddr(const char* p){ return (u32)(size_t)(const LAS char*)p; }

constexpr int TILE_B   = 512;
constexpr int CAPF     = 24;
constexpr int CAP_T    = CAPF+1;
constexpr int ACODE0   = 0x3E80;
constexpr int ACODEG0  = 0x3800;
constexpr int ACODE1   = 0x40A0;
constexpr int NALDS    = ACODE1-ACODE0;
constexpr int NACODE   = ACODE1-ACODEG0;
constexpr int XROW     = 640;
constexpr int NBK      = 256;

#define WT_AUX 16
typedef __amdgpu_buffer_rsrc_t rsrc_t;
DI rsrc_t mkrsrc(void* p, u32 bytes){ return __builtin_amdgcn_make_buffer_rsrc(p, 0, bytes, 0x00020000); }
DI void st16(rsrc_t r, u32 off, u32x4 v){ __builtin_amdgcn_raw_buffer_store_b128(v, r, off, 0, WT_AUX); }
DI int sld(const int* p){ return *(const __attribute__((address_space(4))) int*)p; }

#define K2_PRIO 2

struct PrepArgs {
  const u16* sv; const u16* pos; const int* src; const int* dst; const u16* freqs; const u16* w1; const u16* w2; const u16* wr;
  u32* rec; u32* desc; int* blkmax; u16* Atab; int* misc; char* X; char* img; int* ntiles; int* gnode; int* slow; u32* wfrag; unsigned long long* stamps;
  int N; int E; int BS1; int CH; u32 bsmagic; u32 wkey;
};

template<int CTRL, int RM> DI float dppf(float v){ return asf((u32)__builtin_amdgcn_update_dpp(0, (int)asu(v), CTRL, RM, 0xf, true)); }
template<int CTRL, int RM> DI u32 dppu(u32 v){ return (u32)__builtin_amdgcn_update_dpp(0, (int)v, CTRL, RM, 0xf, true); }
DI float sum32(float v){ v += dppf<0x111,0xf>(v); v += dppf<0x112,0xf>(v); v += dppf<0x114,0xf>(v); v += dppf<0x118,0xf>(v); return v + dppf<0x142,0xa>(v); }
DI float max32(float v){ v = fmaxf(v,dppf<0x111,0xf>(v)); v = fmaxf(v,dppf<0x112,0xf>(v)); v = fmaxf(v,dppf<0x114,0xf>(v)); v = fmaxf(v,dppf<0x118,0xf>(v)); return fmaxf(v,dppf<0x142,0xa>(v)); }
DI float sum64(float v){ v = sum32(v); return v + dppf<0x143,0xc>(v); }
DI float max64(float v){ v = max32(v); return fmaxf(v,dppf<0x143,0xc>(v)); }
DI u32 scan64(u32 v){ v += dppu<0x111,0xf>(v); v += dppu<0x112,0xf>(v); v += dppu<0x114,0xf>(v); v += dppu<0x118,0xf>(v); v += dppu<0x142,0xa>(v); v += dppu<0x143,0xc>(v); return v; }
DI u32 scan16(u32 v){ v += dppu<0x111,0xf>(v); v += dppu<0x112,0xf>(v); v += dppu<0x114,0xf>(v); v += dppu<0x118,0xf>(v); return v; }
DI int exp_ceil(float x){ return __builtin_amdgcn_frexp_expf(x); }
DI float maxabs8(u32x4 x){ float m = 0.f;
#pragma unroll
  for (int k=0;k<4;k++){ const u32 v = x[k]; m = fmaxf(m, fmaxf(fabsf(bf2f(v&0xffff)), fabsf(asf(v&0xffff0000u)))); }
  return m; }
DI float sumsq8(u32x4 x){ float m = 0.f;
#pragma unroll
  for (int k=0;k<4;k++){ const u32 v = x[k]; const float a = bf2f(v&0xffff), b = asf(v&0xffff0000u); m = fmaf(a,a,fmaf(b,b,m)); }
  return m; }

constexpr int K1_SCR = 2048 + 256;
template<int STAMP>
__global__ __launch_bounds__(1024) void k1_partition(PrepArgs p){
  extern __shared__ __attribute__((aligned(16))) char smem[];
  const int tid = threadIdx.x, lane = tid&63, w = __builtin_amdgcn_readfirstlane(tid>>6), b = blockIdx.x;
  unsigned long long t0=0, ts[6]={0,0,0,0,0,0}; if (STAMP) t0 = wall_clock64();
  const int posb = p.N*6, scr = (posb + 1023) & ~1023;
  u32* hist = (u32*)(smem + scr);
  u32* offs = hist + 256;
  float* red = (float*)(offs + 256);
  const int nb0 = b*p.BS1;
  const int e0 = b*p.CH + 4*tid;
  int sj[4], dj[4];
  const bool mine = 4*tid < p.CH;
  if (mine && e0 + 3 < p.E) { const i32x4 s4 = *(const i32x4*)(p.src + e0), d4 = *(const i32x4*)(p.dst + e0);
#pragma unroll
    for (int k=0;k<4;k++){ sj[k]=s4[k]; dj[k]=d4[k]; } }
  else {
#pragma unroll
    for (int k=0;k<4;k++){ const bool ok = mine && e0+k < p.E; sj[k] = ok ? p.src[e0+k] : -1; dj[k] = ok ? p.dst[e0+k] : 0; } }
  if (tid < 256) hist[tid] = 0;
  if (tid < 2) *(u32*)(red + 33 + tid) = 0;
  u32x4 rw[4];
#pragma unroll
  for (int i=0;i<4;i++){ const int ln = 2*(w + 16*i) + (lane>>5); const int node = min(nb0 + min(ln, p.BS1-1), p.N-1); rw[i] = *(const u32x4*)((const char*)p.sv + (size_t)node*512 + (lane&31)*16); }
  u32x4 wk[8];
  if (w == 15 && b >= 1 && b <= 6) {
#pragma unroll
    for (int j=0;j<8;j++){ const u16* q = b == 1 ? p.w1 + 8*(lane + 64*j) : b == 2 ? p.wr + 8*(lane + 64*min(j,5)) : p.w2 + (16*(b-3) + 8*min(lane/24, 1) + j)*192 + 8*(lane%24); wk[j] = *(const u32x4*)q; } }
  const int nck = (posb + 1023) >> 10, shr = (nck + 31) >> 5, own0 = ((b>>3) & 31)*shr;
  for (int i = w; i < shr; i += 16) { const int ck = own0 + i, off = ck*1024 + lane*16; if (off + 16 <= posb) dma16((const char*)p.pos + off, smem + ck*1024); }
  WAITVM(0);
  if (STAMP) ts[4] = wall_clock64();
  { int ck = (w + b*37) % nck;
    for (int i = w; i < nck; i += 16) { const int off = ck*1024 + lane*16; if ((u32)(ck - own0) >= (u32)shr && off + 16 <= posb) dma16((const char*)p.pos + off, smem + ck*1024); ck += 16; ck -= ck >= nck ? nck : 0; } }
  if (tid < ((posb & 15)>>1)) ((u16*)(smem + (posb & ~15)))[tid] = p.pos[((posb & ~15)>>1) + tid];
  if (w == 15 && b >= 1 && b <= 6) {
    if (b == 1) { float s1 = 0.f;
#pragma unroll
      for (int i=0;i<8;i++) s1 += sumsq8(wk[i]);
      s1 = sum64(s1); if (lane == 63) p.misc[2] = (int)asu(s1); }
    else if (b == 2) { float m = 0.f;
#pragma unroll
      for (int i=0;i<6;i++) m = fmaxf(m, maxabs8(wk[i]));
      m = max64(m); const float fs = sum64(lane < 16 ? fabsf(bf2f(p.freqs[lane])) : 0.f);
      if (lane == 63) p.misc[0] = max(-110, min(110, 10 - exp_ceil(1.05f*fs*m + 1e-36f))); }
    else { float cs[8] = {0,0,0,0,0,0,0,0};
#pragma unroll
      for (int j=0;j<8;j++)
#pragma unroll
        for (int k=0;k<4;k++){ const u32 v = wk[j][k]; const float lo = bf2f(v&0xffff), hi = asf(v&0xffff0000u); cs[2*k] = fmaf(lo,lo,cs[2*k]); cs[2*k+1] = fmaf(hi,hi,cs[2*k+1]); }
      float m = 0.f;
#pragma unroll
      for (int k=0;k<8;k++) m = fmaxf(m, cs[k]);
      const float m0 = max64(lane < 24 ? m : 0.f), m1 = max64(lane >= 24 && lane < 48 ? m : 0.f);
      if (lane == 63) p.misc[b] = (int)asu(m0 + m1); }
  }
  {
    typedef __attribute__((ext_vector_type(2))) unsigned short us2;
    us2 m2 = {0,0};
#pragma unroll
    for (int i=0;i<4;i++)
#pragma unroll
      for (int k=0;k<4;k++){ const u32 x = rw[i][k] & 0x7fff7fffu; m2 = __builtin_elementwise_max(m2, __builtin_bit_cast(us2, x)); }
    const u32 m = max((u32)m2.x, (u32)m2.y);
    const bool iss = (lane&31) < 8;
    u32 ms = iss ? m : 0u, mv = iss ? 0u : m;
    ms = max(ms, dppu<0x111,0xf>(ms)); ms = max(ms, dppu<0x112,0xf>(ms)); ms = max(ms, dppu<0x114,0xf>(ms)); ms = max(ms, dppu<0x118,0xf>(ms));
    mv = max(mv, dppu<0x111,0xf>(mv)); mv = max(mv, dppu<0x112,0xf>(mv)); mv = max(mv, dppu<0x114,0xf>(mv)); mv = max(mv, dppu<0x118,0xf>(mv));
    __builtin_amdgcn_s_waitcnt(0xC07F); __builtin_amdgcn_s_barrier();
    if ((lane&15) == 15) { atomicMax((u32*)(red + 33), ms); atomicMax((u32*)(red + 34), mv); } }
  if (STAMP) { WAITVM(0); ts[5] = wall_clock64(); if (lane == 0) p.stamps[4096 + b*16 + w] = ts[5]; }
  __syncthreads();
  if (STAMP) ts[0] = wall_clock64();
  if (tid == 0) p.blkmax[b] = (int)(*(u32*)(red + 33) | (*(u32*)(red + 34) << 16));
  if (w >= 13) {
    const int u = tid - 832;
    constexpr int per = (NACODE + NBK - 1)/NBK; const int code = b*per + (u>>4), r = u&15;
    if (u < per*16 && code < NACODE) { const float d = bf2f(code + ACODEG0);
      const float fc = rbf(0.5f*rbf(rbf(cosf(rbf(rbf(3.141592653589793f*d)/5.0f)))+1.0f));
      const float rb_ = rbf(rbf(sinf(rbf(d*bf2f(p.freqs[r]))))/d);
      p.Atab[code*16 + r] = (u16)pk2(rb_*fc, 0.f); }
  }
  u32 r0[4], r1[4], r2[4]; int bk[4]; u32 rank[4];
  if (mine) {
#pragma unroll
    for (int k=0;k<4;k++){
      const int s = max(sj[k],0), d = dj[k];
      const u32* ps = (const u32*)(smem + ((6*s)&~3)); const u32* pd = (const u32*)(smem + ((6*d)&~3));
      const u32 s0 = ps[0], s1 = ps[1], d0 = pd[0], d1 = pd[1];
      const u32 shs = (s&1)*16, shd = (d&1)*16;
      const u32 sa = __builtin_amdgcn_alignbit(s1, s0, shs), sb = s1 >> shs;
      const u32 da = __builtin_amdgcn_alignbit(d1, d0, shd), db = d1 >> shd;
      const u32 rxy = pk2(bf2f(sa&0xffff) - bf2f(da&0xffff), asf(sa&0xffff0000u) - asf(da&0xffff0000u));
      const float rx = asf(rxy<<16), ry = asf(rxy&0xffff0000u), rz = rbf(asf(sb<<16) - asf(db<<16));
      const u32 sq = pk2(rx*rx, ry*ry);
      const float ss = rbf(asf(sq<<16) + asf(sq&0xffff0000u) + rbf(rz*rz));
      const float dd = rbf(__builtin_amdgcn_sqrtf(rbf(ss + 1e-8f)));
      const bool keep = sj[k] >= 0 && dd < 5.0f;
      const float inv = __builtin_amdgcn_rcpf(dd);
      const u32 bucket = __umulhi((u32)d, p.bsmagic), dl = d - bucket*p.BS1;
      r0[k] = (u32)s | (dl<<24); r1[k] = (asu(dd)>>16) | (pk2(rx*inv, 0.f)<<16); r2[k] = pk2(ry*inv, rz*inv);
      bk[k] = keep ? (int)bucket : -1;
      rank[k] = keep ? atomicAdd(&hist[bucket], 1u) : 0u;
    }
  } else {
#pragma unroll
    for (int k=0;k<4;k++){ bk[k] = -1; r0[k] = r1[k] = r2[k] = rank[k] = 0; }
  }
  __syncthreads();
  if (STAMP) ts[1] = wall_clock64();
  if (w == 0) { const u32x4 c = *(const u32x4*)(hist + 4*lane); const u32 s = c[0]+c[1]+c[2]+c[3];
    const u32 inc = scan64(s); u32 ex = inc - s; u32x4 o;
#pragma unroll
    for (int k=0;k<4;k++){ o[k] = ex; p.desc[(4*lane+k)*NBK + b] = ex | (c[k]<<16); ex += c[k]; }
    *(u32x4*)(offs + 4*lane) = o;
    if (lane == 63) red[32] = asf(inc);
  }
  __syncthreads();
  if (STAMP) ts[2] = wall_clock64();
  u32* stg = (u32*)smem;
#pragma unroll
  for (int k=0;k<4;k++) if (bk[k] >= 0) { const u32 q = 3*(offs[bk[k]] + rank[k]); stg[q] = r0[k]; stg[q+1] = r1[k]; stg[q+2] = r2[k]; }
  __syncthreads();
  if (STAMP) ts[3] = wall_clock64();
  const int nch = (asu(red[32])*12 + 15) >> 4;
  const rsrc_t ro = mkrsrc((char*)p.rec + (size_t)b*p.CH*12, p.CH*12);
  for (int i = tid; i < nch; i += 1024) st16(ro, i*16, ((const u32x4*)smem)[i]);
  if (STAMP) { WAITVM(0); if (tid == 0) { p.stamps[b*2] = t0; p.stamps[b*2+1] = wall_clock64(); for (int i=0;i<6;i++) p.stamps[1024 + b*8 + i] = ts[i]; } }
}

constexpr int K15_SORT = 4*CAP_T*32*16 + 4*CAP_T*128 + 1024 + 320 + 256 + 64 + 576 + 128 + 8192, K15_TAB = 32768 + 8*32*144;
constexpr int K15_LDS = K15_SORT > K15_TAB ? K15_SORT : K15_TAB;
DI u32 h16(float x){ return pkh(x,0.f)&0xffff; }
template<int STAMP>
__global__ __launch_bounds__(512) void k15_sort_table(PrepArgs p){
  extern __shared__ __attribute__((aligned(16))) char smem[];
  const int tid = threadIdx.x, lane = tid&63, w = __builtin_amdgcn_readfirstlane(tid>>6);
  unsigned long long t0=0, ts[6]={0,0,0,0,0,0}; if (STAMP) t0 = wall_clock64();
  if (blockIdx.x < NBK) {
    const int kb = blockIdx.x;
    u32x4* aos = (u32x4*)smem;
    u32* bmp = (u32*)(smem + 4*CAP_T*32*16);
    u32* trash = bmp + 4*CAP_T*32;
    u32* cnt = trash + 256;
    u32* base = cnt + 80;
    int* gt = (int*)(base + 64);
    u32* ncnt = (u32*)(gt + 16);
    unsigned char* nmap = (unsigned char*)(ncnt + 144);
    const int sg = tid>>4, i16 = tid&15;
    u32 ds[8];
#pragma unroll
    for (int it=0;it<8;it++) ds[it] = p.desc[kb*NBK + sg + 32*it];
    for (int i = tid; i < 4*CAP_T*(32*16 + 128)/16; i += 512) ((u32x4*)smem)[i] = u32x4{0,0,0,0};
    if (tid < 80) cnt[tid] = 0;
    if (tid < 144) ncnt[tid] = 0;
    if (tid == 0) gt[4] = 0;
    u32x3 ra[8], rb[8]; bool va[8], vb[8]; bool over = false, anyb = false;
#pragma unroll
    for (int it=0;it<8;it++){ const u32 c = ds[it]>>16, o = ds[it]&0xffff; const u32 q = (u32)(sg + 32*it)*p.CH + o;
      va[it] = (u32)i16 < c; vb[it] = (u32)i16 + 16 < c; over |= c > 32; anyb |= vb[it];
      ra[it] = *(const u32x3*)(p.rec + 3*(q + (va[it] ? i16 : 0))); rb[it] = u32x3{0,0,0}; }
    const bool wb = __any(anyb);
    if (wb) {
#pragma unroll
      for (int it=0;it<8;it++){ const u32 o = ds[it]&0xffff; const u32 q = (u32)(sg + 32*it)*p.CH + o; if (vb[it]) rb[it] = *(const u32x3*)(p.rec + 3*(q + i16 + 16)); } }
    __builtin_amdgcn_s_waitcnt(0xC07F);
    __builtin_amdgcn_s_barrier();
    if (STAMP) { WAITVM(0); ts[0] = wall_clock64(); }
#pragma unroll
    for (int it=0;it<8;it++) atomicAdd(&ncnt[va[it] ? ra[it].x>>24 : 128u + i16], 1u);
    if (wb) {
#pragma unroll
      for (int it=0;it<8;it++) atomicAdd(&ncnt[vb[it] ? rb[it].x>>24 : 128u + i16], 1u); }
    __syncthreads();
    if (w == 0) { const u32 c0 = ncnt[2*lane], c1 = ncnt[2*lane+1], inc = scan64(c0 + c1);
      const u32 tot = (u32)__builtin_amdgcn_readlane((int)inc, 63), e0 = inc - c0 - c1, e1 = e0 + c0;
      u32 g0 = tot ? min(3u, (4*e0 + 2*c0)/tot) : 0, g1 = tot ? min(3u, (4*e1 + 2*c1)/tot) : 0;
      u32 st[5]; st[0] = 0; st[4] = p.BS1; bool ok = true;
#pragma unroll
      for (int j=1;j<4;j++) st[j] = __builtin_popcountll(__ballot(2*lane < p.BS1 && g0 < (u32)j)) + __builtin_popcountll(__ballot(2*lane+1 < p.BS1 && g1 < (u32)j));
#pragma unroll
      for (int j=0;j<4;j++) ok &= st[j+1] - st[j] <= 31u;
      if (!ok) {
#pragma unroll
        for (int j=1;j<4;j++) st[j] = (j*(u32)p.BS1)>>2;
        g0 = (2*lane >= st[1]) + (2*lane >= st[2]) + (2*lane >= st[3]); g1 = (2*lane+1 >= st[1]) + (2*lane+1 >= st[2]) + (2*lane+1 >= st[3]); }
      const u32 s0_ = g0 == 0 ? st[0] : g0 == 1 ? st[1] : g0 == 2 ? st[2] : st[3], s1_ = g1 == 0 ? st[0] : g1 == 1 ? st[1] : g1 == 2 ? st[2] : st[3];
      ((u16*)nmap)[lane] = (u16)(((g0<<5) | ((2*lane - s0_)&31)) | (((g1<<5) | ((2*lane+1 - s1_)&31))<<8));
      if (lane < 4) { const u32 a_ = lane == 0 ? st[0] : lane == 1 ? st[1] : lane == 2 ? st[2] : st[3], b_ = lane == 0 ? st[1] : lane == 1 ? st[2] : lane == 2 ? st[3] : st[4];
        p.gnode[kb*4 + lane] = (int)(a_ | ((b_ - a_)<<8)); } }
    __syncthreads();
    u32 ba[8], bb[8], ka[8], kb_[8], na[8], nb[8];
    auto classify = [&](const u32x3& r, bool v, u32& bin, u32& rk, u32& n){ const u32 src = r.x&0xffffff, m = nmap[(r.x>>24)&127]; n = m&31;
      bin = v ? (m>>5)*16 + min(15u, __umulhi(src, p.wkey)) : 64u + i16; rk = atomicAdd(&cnt[bin],1u); };
#pragma unroll
    for (int it=0;it<8;it++) classify(ra[it], va[it], ba[it], ka[it], na[it]);
    if (wb) {
#pragma unroll
      for (int it=0;it<8;it++) classify(rb[it], vb[it], bb[it], kb_[it], nb[it]); }
    if (over) gt[4] = 1;
    __syncthreads();
    if (STAMP) ts[1] = wall_clock64();
    if (w == 0) { const u32 c = cnt[lane], inc = scan16(c);
      base[lane] = inc - c;
      if ((lane&15) == 15) { const int nt = max(1,(int)((inc+31)>>5)); gt[lane>>4] = nt; if (nt > CAPF) gt[4] = 1; } }
    __syncthreads();
    if (STAMP) ts[2] = wall_clock64();
    const bool ovf = gt[4] != 0;
    if (!ovf) {
      auto scatter = [&](const u32x3& r, u32 bin, u32 rk, u32 n){ const bool v = bin < 64; const u32 src = r.x&0xffffff, j = bin>>4;
        const u32 pos = base[bin&63] + rk, rho = pos&31, pp = (rho&~12u) | ((rho&4)<<1) | ((rho&8)>>1);
        const u32 code = r.y&0xffff, sel = code >= (u32)ACODE0 ? (code-ACODE0)*32 : (0x8000u | (code-ACODEG0));
        u32x4 o; o[0] = src*XROW; o[1] = sel; o[2] = pkh(asf(r.y&0xffff0000u), bf2f(r.z&0xffff)); o[3] = pkh(asf(r.z&0xffff0000u), 0.f);
        u32x4* d = v ? aos + j*(CAP_T*32) + pos : (u32x4*)trash + (lane&63); *d = o;
        atomicOr(v ? bmp + (j*CAP_T + (pos>>5))*32 + n : trash + 4*(lane&63), 1u<<pp); };
#pragma unroll
      for (int it=0;it<8;it++) scatter(ra[it], ba[it], ka[it], na[it]);
      if (wb) {
#pragma unroll
        for (int it=0;it<8;it++) scatter(rb[it], bb[it], kb_[it], nb[it]); }
    }
    __syncthreads();
    if (STAMP) ts[3] = wall_clock64();
    if (tid < 4) p.ntiles[kb*4 + tid] = ovf ? 1 : gt[tid];
    if (tid == 0) p.slow[kb] = ovf ? 1 : 0;
    { const int rho = lane&31, pp = (rho&~12) | ((rho&4)<<1) | ((rho&8)>>1), pi = (rho&3)*8 + 2*(rho>>3) + ((rho>>2)&1);
      int tot = 0, st[5];
#pragma unroll
      for (int j=0;j<4;j++){ st[j] = tot; tot += (ovf ? 1 : gt[j]) + 1; }
      st[4] = tot;
      const rsrc_t o = mkrsrc(p.img + (size_t)kb*4*CAP_T*TILE_B, 4*CAP_T*TILE_B);
      char* stage = (char*)(nmap + 128) + w*1024;
      for (int q = 2*w + (lane>>5); q < tot; q += 16) {
        const int j = (q >= st[1]) + (q >= st[2]) + (q >= st[3]), t = q - (j == 0 ? 0 : j == 1 ? st[1] : j == 2 ? st[2] : st[3]);
        const u32x4 r = ovf ? u32x4{0,0,0,0} : aos[j*(CAP_T*32) + t*32 + rho]; const u32 bm = ovf ? 0u : bmp[(j*CAP_T + t)*32 + rho];
        char* im = stage + (lane>>5)*TILE_B;
        ((u32*)im)[pi] = r[0]; ((u16*)(im+128))[rho] = (u16)r[1]; ((u32*)(im+192))[rho] = bm;
        ((u16*)(im+320))[pp] = (u16)r[2]; ((u16*)(im+384))[pp] = (u16)(r[2]>>16); ((u16*)(im+448))[pp] = (u16)r[3];
        WAITLG0();
        const u32x4 v = *(const u32x4*)(stage + lane*16);
        st16(o, (j*CAP_T + t)*TILE_B + (lane&31)*16, v);
        WAITLG0();
      } }
  } else {
    const int tb = blockIdx.x - NBK;
    char* W1 = smem; char* W2 = smem + 8192;
    const int own = (tb>>3) & 31;
    if (w == 0) dma16((own < 8 ? (const char*)p.w1 + own*1024 : (const char*)p.w2 + (own-8)*1024) + lane*16, smem + own*1024);
    const int per = (p.BS1 + 3)>>2;
    const int h = lane>>5, c = lane&31, tl = w&3, hf = w>>2;
    const int ln = tl*per + min(c, per-1);
    const int node = min(tb*p.BS1 + min(ln, p.BS1-1), p.N-1);
    const char* row = (const char*)p.sv + (size_t)node*512;
    u32x4 sf[4]; u32x2 vv[3][4];
#pragma unroll
    for (int ks=0;ks<4;ks++) sf[ks] = *(const u32x4*)(row + ks*32 + h*16);
#pragma unroll
    for (int k=0;k<3;k++)
#pragma unroll
      for (int m=0;m<4;m++) vv[k][m] = *(const u32x2*)(row + 128*(1+k) + 64*hf + 16*m + 8*h);
    const u32x4 bm = *(const u32x4*)(p.blkmax + 4*lane);
    const float c1 = asf((u32)p.misc[2]), c2 = asf((u32)p.misc[3]) + asf((u32)p.misc[4]) + asf((u32)p.misc[5]) + asf((u32)p.misc[6]);
    WAITVM(0);
    for (int i = w; i < 32; i += 8) { const int ck = (i + tb*5) & 31; if (ck != own) dma16((ck < 8 ? (const char*)p.w1 + ck*1024 : (const char*)p.w2 + (ck-8)*1024) + lane*16, smem + ck*1024); }
    float scl;
    { u32 ms = 0, mv = 0;
#pragma unroll
      for (int k=0;k<4;k++){ ms = max(ms, bm[k]&0xffff); mv = max(mv, bm[k]>>16); }
      const float fs = asf((u32)__builtin_amdgcn_readlane((int)asu(max64(bf2f(ms))), 63)), fv = asf((u32)__builtin_amdgcn_readlane((int)asu(max64(bf2f(mv))), 63));
      const int s0 = max(-110, min(110, 4 - exp_ceil(sqrtf(c1)*sqrtf(c2)*8.8f*fs*fmaxf(1.f,fv) + 1e-36f)));
      if (tb == 0 && tid == 0) p.misc[1] = s0;
      scl = ldexpf(1.f, s0); }
    if (tb == 1 && w < 6) {
      const float wsc = ldexpf(1.f, p.misc[0]); const int col = (w < 4 ? 64 + 64*(w&1) + 32*(w>>1) : 32*(w-4)) + c; u32x4 f;
#pragma unroll
      for (int j=0;j<4;j++) f[j] = pk2(bf2f(p.wr[(8*h+2*j)*192 + col])*wsc, bf2f(p.wr[(8*h+2*j+1)*192 + col])*wsc);
      *(u32x4*)(p.wfrag + (w*64 + lane)*4) = f; }
    WAITVM(0);
    __syncthreads();
    if (STAMP) ts[0] = wall_clock64();
    const int blk = (lane>>4)&1, q = (lane&15)>>2, pp = lane&3;
    f32x16 z;
#pragma unroll
    for (int r=0;r<16;r++) z[r] = 0.f;
    u32x4 hb[2][2];
#pragma unroll
    for (int jt=0;jt<2;jt++){ f32x16 x = z;
#pragma unroll
      for (int ks=0;ks<4;ks++){ const char* a = W1 + (16*ks + 8*h + q)*128 + (32*jt + 16*blk + 4*pp)*2; const u32x2 t0_ = trread(a), t1_ = trread(a + 4*128);
        x = mfma32(u32x4{t0_.x,t0_.y,t1_.x,t1_.y}, sf[ks], x); }
      float hh[16];
#pragma unroll
      for (int r=0;r<16;r++){ const float xr = rbf(x[r]); hh[r] = xr*__builtin_amdgcn_rcpf(1.f + __expf(-xr)); }
#pragma unroll
      for (int s=0;s<2;s++)
#pragma unroll
        for (int j=0;j<4;j++) hb[jt][s][j] = pk2(hh[8*s+2*j], hh[8*s+2*j+1]);
    }
    if (STAMP) ts[1] = wall_clock64();
    auto phi_tile = [&](int ct){ f32x16 x = z;
#pragma unroll
      for (int jt=0;jt<2;jt++)
#pragma unroll
        for (int s=0;s<2;s++){ const char* a = W2 + (32*jt + 16*s + 4*h + q)*384 + (32*ct + 16*blk + 4*pp)*2; const u32x2 t0_ = trread(a), t1_ = trread(a + 8*384);
          x = mfma32(u32x4{t0_.x,t0_.y,t1_.x,t1_.y}, hb[jt][s], x); }
#pragma unroll
      for (int r=0;r<16;r++) x[r] = rbf(x[r]);
      return x; };
    char* stg = smem + 32768 + w*32*144;
    const int nrow = max(0, min(per, min(p.BS1 - tl*per, p.N - (tb*p.BS1 + tl*per))));
    const rsrc_t xr = mkrsrc(p.X + (size_t)min(tb*p.BS1 + tl*per, p.N)*XROW, nrow*XROW);
    char* d = stg + c*144 + 8*h;
    auto put = [&](int seg, int m, float a0, float a1, float a2, float a3){ u32x2 o; o.x = pkh(a0*scl, a1*scl); o.y = pkh(a2*scl, a3*scl); *(u32x2*)(d + 64*seg + 16*m) = o; };
    auto flush = [&](int off){ WAITLG0();
#pragma unroll
      for (int i=0;i<4;i++){ const int r = 8*i + (lane>>3); st16(xr, r*XROW + off + (lane&7)*16, *(const u32x4*)(stg + r*144 + (lane&7)*16)); }
      WAITLG0(); };
    const f32x16 pv = phi_tile(2+hf);
#pragma unroll
    for (int k=0;k<2;k++)
#pragma unroll
      for (int m=0;m<4;m++){ const u32x2 v = vv[k][m]; put(k, m, pv[4*m]*bf2f(v.x&0xffff), pv[4*m+1]*asf(v.x&0xffff0000u), pv[4*m+2]*bf2f(v.y&0xffff), pv[4*m+3]*asf(v.y&0xffff0000u)); }
    const f32x16 pr = phi_tile(4+hf);
    flush(hf*256);
#pragma unroll
    for (int m=0;m<4;m++){ const u32x2 v = vv[2][m]; put(0, m, pv[4*m]*bf2f(v.x&0xffff), pv[4*m+1]*asf(v.x&0xffff0000u), pv[4*m+2]*bf2f(v.y&0xffff), pv[4*m+3]*asf(v.y&0xffff0000u));
      put(1, m, pr[4*m], pr[4*m+1], pr[4*m+2], pr[4*m+3]); }
    const f32x16 ps = phi_tile(hf);
    flush(hf*256 + 128);
    if (STAMP) ts[2] = wall_clock64();
#pragma unroll
    for (int m=0;m<4;m++) put(0, m, ps[4*m], ps[4*m+1], ps[4*m+2], ps[4*m+3]);
    WAITLG0();
#pragma unroll
    for (int i=0;i<2;i++){ const int r = 16*i + (lane>>2); st16(xr, r*XROW + 512 + hf*64 + (lane&3)*16, *(const u32x4*)(stg + r*144 + (lane&3)*16)); }
  }
  if (STAMP) { const unsigned long long te = wall_clock64(); WAITVM(0); __syncthreads(); if (tid == 0) { p.stamps[blockIdx.x*2] = t0; p.stamps[blockIdx.x*2+1] = wall_clock64(); ts[5] = te; for (int i=0;i<6;i++) p.stamps[1024 + blockIdx.x*8 + i] = ts[i]; } }
}

#define SB() __builtin_amdgcn_sched_barrier(0)
constexpr int REC_S = 384;
constexpr int L_ATAB   = 0;
constexpr int L_LUT    = NALDS*32;
constexpr int L_VRING  = L_LUT + 4096;
constexpr int L_SRING  = L_VRING + 8*3*4096;
constexpr int L_VREC   = L_SRING + 4*2*2048;
constexpr int L_RES    = L_VREC + 8*3*TILE_B;
constexpr int K2_LDS   = L_RES + 8*1920;

struct K2Args {
  const u16* sv; const u16* wr; const char* X; const char* AtabG; const char* img; const int* ntiles; u16* out;
  const u32* rec; const u32* desc; const int* misc; const int* slow; const int* gnode; const u32* wfrag; unsigned long long* stamps;
  int N; int BS1; int CH; int prio;
};

DI void k2_slow(const K2Args& p, char* smem, int bk, int hf){
  float* accV = (float*)smem;
  float* accS = accV + 2*p.BS1*96;
  const int tid = threadIdx.x, lane = tid&63, w = tid>>6;
  for (int i = tid; i < 2*p.BS1*96 + p.BS1*64; i += 768) accV[i] = 0.f;
  __syncthreads();
  const int s0 = p.misc[1];
  for (int cell = w; cell < 2*NBK; cell += 12) {
    const int kb = 2*bk + (cell >= NBK ? 1 : 0), blk = cell & (NBK-1);
    const u32 ds = p.desc[kb*NBK + blk], cnt = ds>>16, off = ds&0xffff;
    for (u32 i = 0; i < cnt; i++) {
      const u32* r = p.rec + 3*((size_t)blk*p.CH + off + i);
      const u32 r0 = r[0], r1 = r[1], r2 = r[2];
      u32 src = r0&0xffffff; PIN(src);
      const u32 dl = r0>>24;
      const u16* arow = (const u16*)p.AtabG + ((r1&0xffff) - ACODEG0)*16;
      const u32 xo = src*XROW;
      const _Float16* x = (const _Float16*)(p.X + xo);
      const int ex = -s0;
      if (lane < 32) { float wv = 0.f, wrr = 0.f;
        for (int q = 0; q < 16; q++) { const float a = bf2f(arow[q]); wv += a*bf2f(p.wr[q*192 + 64 + 32*hf + lane]); wrr += a*bf2f(p.wr[q*192 + 128 + 32*hf + lane]); }
        const float mr = wrr*(float)x[hf*128 + 96 + lane];
        float* d = accV + ((kb-2*bk)*p.BS1 + dl)*96 + lane;
        atomicAdd(d,    ldexpf(wv*(float)x[hf*128 + lane]      + mr*asf(r1&0xffff0000u), ex));
        atomicAdd(d+32, ldexpf(wv*(float)x[hf*128 + 32 + lane] + mr*bf2f(r2&0xffff), ex));
        atomicAdd(d+64, ldexpf(wv*(float)x[hf*128 + 64 + lane] + mr*asf(r2&0xffff0000u), ex)); }
      if (kb == 2*bk + hf) { float ws = 0.f;
        for (int q = 0; q < 16; q++) ws += bf2f(arow[q])*bf2f(p.wr[q*192 + lane]);
        atomicAdd(accS + dl*64 + lane, ldexpf(ws*(float)x[256 + lane], ex)); }
    }
  }
  __syncthreads();
  for (int i = tid; i < 2*p.BS1*96; i += 768) { const int n = i/96, k = (i%96)>>5, c = i&31; const int node = 2*bk*p.BS1 + n;
    if (node < p.N) { const size_t o = (size_t)node*256 + 64*(1+k) + 32*hf + c; p.out[o] = (u16)pk2(bf2f(p.sv[o]) + accV[i], 0.f); } }
  for (int i = tid; i < p.BS1*64; i += 768) { const int node = (2*bk+hf)*p.BS1 + (i>>6);
    if (node < p.N) { const size_t o = (size_t)node*256 + (i&63); p.out[o] = (u16)pk2(bf2f(p.sv[o]) + accS[i], 0.f); } }
}

DI void fix_A(u32x4& a, u32 sel, const char* AtabG){
  if (__builtin_expect(__any(sel>=0x8000u),0)) {
    u32 go = sel; PIN(go);
    go = (go>=0x8000u ? (go&0x7fff)*32 : 0) + ((__lane_id()>>5)<<4);
    u32x4 g;
    asm volatile("global_load_dwordx4 %0, %1, %2\n\ts_waitcnt vmcnt(0)" : "=&v"(g) : "v"(go), "s"(AtabG) : "memory");
    if (sel>=0x8000u) a = g;
  }
}

#define K2_V_LOOP_TEXT \
    "s_nop 1\n\t" \
    "v_and_b32 v139, 0xff, v136\n\t" \
    "v_lshlrev_b32 v139, 4, v139\n\t" \
    "ds_read_b128 v[124:127], v144 offset:448\n\t" \
    "ds_read_b128 v[116:119], v144 offset:320\n\t" \
    "ds_read_b128 v[120:123], v144 offset:384\n\t" \
    "ds_read_b128 v[112:115], v139 offset:17408\n\t" \
    "s_cmp_lg_u32 s40, 0\n\t" \
    "s_cbranch_scc1 LB_LOOP_%=\n\t" \
    "LA_LOOP_%=:\n\t" \
    "ds_read_b64_tr_b16 v[104:105], v143 offset:0\n\t" \
    "ds_read_b64_tr_b16 v[106:107], v143 offset:2048\n\t" \
    "v_mfma_f32_32x32x16_bf16 v[64:79], v[88:91], v[84:87], 0\n\t" \
    "ds_read_b64_tr_b16 v[100:101], v142 offset:0\n\t" \
    "ds_read_b64_tr_b16 v[102:103], v142 offset:2048\n\t" \
    "ds_read_b64_tr_b16 v[92:93], v140 offset:0\n\t" \
    "ds_read_b64_tr_b16 v[94:95], v140 offset:2048\n\t" \
    "v_mfma_f32_32x32x16_bf16 v[48:63], v[88:91], v[80:83], 0\n\t" \
    "ds_read_b64_tr_b16 v[96:97], v141 offset:0\n\t" \
    "ds_read_b64_tr_b16 v[98:99], v141 offset:2048\n\t" \
    "ds_read_u16 v137, v145 offset:640\n\t" \
    "ds_read_b32 v138, v146 offset:704\n\t" \
    "s_nop 3\n\t" \
    "v_cvt_pk_f16_f32 v132, v64, v65\n\t" \
    "v_cvt_pk_f16_f32 v133, v66, v67\n\t" \
    "v_cvt_pk_f16_f32 v134, v68, v69\n\t" \
    "v_cvt_pk_f16_f32 v135, v70, v71\n\t" \
    "v_cvt_pk_f16_f32 v128, v48, v49\n\t" \
    "v_cvt_pk_f16_f32 v129, v50, v51\n\t" \
    "v_cvt_pk_f16_f32 v130, v52, v53\n\t" \
    "v_cvt_pk_f16_f32 v131, v54, v55\n\t" \
    "s_waitcnt lgkmcnt(6)\n\t" \
    "v_pk_mul_f16 v104, v132, v104\n\t" \
    "v_pk_mul_f16 v105, v133, v105\n\t" \
    "v_pk_mul_f16 v106, v134, v106\n\t" \
    "v_pk_mul_f16 v107, v135, v107\n\t" \
    "v_pk_mul_f16 v100, v128, v100\n\t" \
    "v_pk_mul_f16 v101, v129, v101\n\t" \
    "v_pk_mul_f16 v102, v130, v102\n\t" \
    "v_pk_mul_f16 v103, v131, v103\n\t" \
    "v_pk_fma_f16 v100, v104, v124, v100\n\t" \
    "v_pk_fma_f16 v101, v105, v125, v101\n\t" \
    "v_pk_fma_f16 v102, v106, v126, v102\n\t" \
    "v_pk_fma_f16 v103, v107, v127, v103\n\t" \
    "s_waitcnt lgkmcnt(2)\n\t" \
    "v_pk_mul_f16 v92, v128, v92\n\t" \
    "v_pk_mul_f16 v93, v129, v93\n\t" \
    "v_mfma_f32_32x32x16_f16 v[32:47], v[112:115], v[100:103], v[32:47]\n\t" \
    "v_pk_mul_f16 v94, v130, v94\n\t" \
    "v_pk_mul_f16 v95, v131, v95\n\t" \
    "v_pk_fma_f16 v92, v104, v116, v92\n\t" \
    "v_pk_fma_f16 v93, v105, v117, v93\n\t" \
    "v_pk_fma_f16 v94, v106, v118, v94\n\t" \
    "v_pk_fma_f16 v95, v107, v119, v95\n\t" \
    "v_pk_mul_f16 v96, v128, v96\n\t" \
    "v_pk_mul_f16 v97, v129, v97\n\t" \
    "v_mfma_f32_32x32x16_f16 v[0:15], v[112:115], v[92:95], v[0:15]\n\t" \
    "v_pk_mul_f16 v98, v130, v98\n\t" \
    "v_pk_mul_f16 v99, v131, v99\n\t" \
    "v_pk_fma_f16 v96, v104, v120, v96\n\t" \
    "v_pk_fma_f16 v97, v105, v121, v97\n\t" \
    "v_pk_fma_f16 v98, v106, v122, v98\n\t" \
    "v_pk_fma_f16 v99, v107, v123, v99\n\t" \
    "s_waitcnt vmcnt(0) lgkmcnt(0)\n\t" \
    "s_nop 0\n\t" \
    "v_mfma_f32_32x32x16_f16 v[16:31], v[112:115], v[96:99], v[16:31]\n\t" \
    "v_lshrrev_b32 v139, 12, v136\n\t" \
    "v_and_b32 v139, 0xff0, v139\n\t" \
    "ds_read_b128 v[124:127], v144 offset:480\n\t" \
    "ds_read_b128 v[116:119], v144 offset:352\n\t" \
    "ds_read_b128 v[120:123], v144 offset:416\n\t" \
    "ds_read_b128 v[112:115], v139 offset:17408\n\t" \
    "s_barrier\n\t" \
    "ds_read_b64_tr_b16 v[104:105], v143 offset:4096\n\t" \
    "ds_read_b64_tr_b16 v[106:107], v143 offset:6144\n\t" \
    "ds_read_b64_tr_b16 v[100:101], v142 offset:4096\n\t" \
    "ds_read_b64_tr_b16 v[102:103], v142 offset:6144\n\t" \
    "ds_read_b64_tr_b16 v[92:93], v140 offset:4096\n\t" \
    "ds_read_b64_tr_b16 v[94:95], v140 offset:6144\n\t" \
    "ds_read_b64_tr_b16 v[96:97], v141 offset:4096\n\t" \
    "ds_read_b64_tr_b16 v[98:99], v141 offset:6144\n\t" \
    "v_and_b32 v147, 0x7fff, v137\n\t" \
    "v_add_u32 v147, v148, v147\n\t" \
    "ds_read_b128 v[88:91], v147\n\t" \
    "v_cvt_pk_f16_f32 v132, v72, v73\n\t" \
    "v_cvt_pk_f16_f32 v133, v74, v75\n\t" \
    "v_cvt_pk_f16_f32 v134, v76, v77\n\t" \
    "v_cvt_pk_f16_f32 v135, v78, v79\n\t" \
    "v_cvt_pk_f16_f32 v128, v56, v57\n\t" \
    "v_cvt_pk_f16_f32 v129, v58, v59\n\t" \
    "v_cvt_pk_f16_f32 v130, v60, v61\n\t" \
    "v_cvt_pk_f16_f32 v131, v62, v63\n\t" \
    "s_waitcnt lgkmcnt(5)\n\t" \
    "v_pk_mul_f16 v104, v132, v104\n\t" \
    "v_pk_mul_f16 v105, v133, v105\n\t" \
    "v_pk_mul_f16 v106, v134, v106\n\t" \
    "v_pk_mul_f16 v107, v135, v107\n\t" \
    "v_pk_mul_f16 v100, v128, v100\n\t" \
    "v_pk_mul_f16 v101, v129, v101\n\t" \
    "v_pk_mul_f16 v102, v130, v102\n\t" \
    "v_pk_mul_f16 v103, v131, v103\n\t" \
    "v_pk_fma_f16 v100, v104, v124, v100\n\t" \
    "v_pk_fma_f16 v101, v105, v125, v101\n\t" \
    "v_pk_fma_f16 v102, v106, v126, v102\n\t" \
    "v_pk_fma_f16 v103, v107, v127, v103\n\t" \
    "s_waitcnt lgkmcnt(1)\n\t" \
    "v_pk_mul_f16 v92, v128, v92\n\t" \
    "v_pk_mul_f16 v93, v129, v93\n\t" \
    "v_mfma_f32_32x32x16_f16 v[32:47], v[112:115], v[100:103], v[32:47]\n\t" \
    "v_pk_mul_f16 v94, v130, v94\n\t" \
    "v_pk_mul_f16 v95, v131, v95\n\t" \
    "v_pk_fma_f16 v92, v104, v116, v92\n\t" \
    "v_pk_fma_f16 v93, v105, v117, v93\n\t" \
    "v_pk_fma_f16 v94, v106, v118, v94\n\t" \
    "v_pk_fma_f16 v95, v107, v119, v95\n\t" \
    "v_pk_mul_f16 v96, v128, v96\n\t" \
    "v_pk_mul_f16 v97, v129, v97\n\t" \
    "v_mfma_f32_32x32x16_f16 v[0:15], v[112:115], v[92:95], v[0:15]\n\t" \
    "v_pk_mul_f16 v98, v130, v98\n\t" \
    "v_pk_mul_f16 v99, v131, v99\n\t" \
    "v_pk_fma_f16 v96, v104, v120, v96\n\t" \
    "v_pk_fma_f16 v97, v105, v121, v97\n\t" \
    "v_pk_fma_f16 v98, v106, v122, v98\n\t" \
    "v_pk_fma_f16 v99, v107, v123, v99\n\t" \
    "s_waitcnt vmcnt(0) lgkmcnt(0)\n\t" \
    "v_cmp_lt_u32 vcc, 0x7fff, v137\n\t" \
    "v_lshrrev_b32 v136, v149, v138\n\t" \
    "s_cbranch_vccnz LV_RARE_A0_%=\n\t" \
    "LV_BACK_A0_%=:\n\t" \
    "v_mfma_f32_32x32x16_f16 v[16:31], v[112:115], v[96:99], v[16:31]\n\t" \
    "v_and_b32 v139, 0xff, v136\n\t" \
    "v_lshlrev_b32 v139, 4, v139\n\t" \
    "ds_read_b128 v[124:127], v144 offset:960\n\t" \
    "ds_read_b128 v[116:119], v144 offset:832\n\t" \
    "ds_read_b128 v[120:123], v144 offset:896\n\t" \
    "ds_read_b128 v[112:115], v139 offset:17408\n\t" \
    "s_barrier\n\t" \
    "s_add_i32 s46, s46, 1\n\t" \
    "s_cmp_eq_u32 s46, s47\n\t" \
    "s_cbranch_scc1 LV_END_%=\n\t" \
    "ds_read_b64_tr_b16 v[104:105], v143 offset:8192\n\t" \
    "ds_read_b64_tr_b16 v[106:107], v143 offset:10240\n\t" \
    "v_mfma_f32_32x32x16_bf16 v[64:79], v[88:91], v[84:87], 0\n\t" \
    "ds_read_b64_tr_b16 v[100:101], v142 offset:8192\n\t" \
    "ds_read_b64_tr_b16 v[102:103], v142 offset:10240\n\t" \
    "ds_read_b64_tr_b16 v[92:93], v140 offset:8192\n\t" \
    "ds_read_b64_tr_b16 v[94:95], v140 offset:10240\n\t" \
    "v_mfma_f32_32x32x16_bf16 v[48:63], v[88:91], v[80:83], 0\n\t" \
    "ds_read_b64_tr_b16 v[96:97], v141 offset:8192\n\t" \
    "ds_read_b64_tr_b16 v[98:99], v141 offset:10240\n\t" \
    "ds_read_u16 v137, v145 offset:1152\n\t" \
    "ds_read_b32 v138, v146 offset:1216\n\t" \
    "s_nop 3\n\t" \
    "v_cvt_pk_f16_f32 v132, v64, v65\n\t" \
    "v_cvt_pk_f16_f32 v133, v66, v67\n\t" \
    "v_cvt_pk_f16_f32 v134, v68, v69\n\t" \
    "v_cvt_pk_f16_f32 v135, v70, v71\n\t" \
    "v_cvt_pk_f16_f32 v128, v48, v49\n\t" \
    "v_cvt_pk_f16_f32 v129, v50, v51\n\t" \
    "v_cvt_pk_f16_f32 v130, v52, v53\n\t" \
    "v_cvt_pk_f16_f32 v131, v54, v55\n\t" \
    "s_waitcnt lgkmcnt(6)\n\t" \
    "v_pk_mul_f16 v104, v132, v104\n\t" \
    "v_pk_mul_f16 v105, v133, v105\n\t" \
    "v_pk_mul_f16 v106, v134, v106\n\t" \
    "v_pk_mul_f16 v107, v135, v107\n\t" \
    "v_pk_mul_f16 v100, v128, v100\n\t" \
    "v_pk_mul_f16 v101, v129, v101\n\t" \
    "v_pk_mul_f16 v102, v130, v102\n\t" \
    "v_pk_mul_f16 v103, v131, v103\n\t" \
    "v_pk_fma_f16 v100, v104, v124, v100\n\t" \
    "v_pk_fma_f16 v101, v105, v125, v101\n\t" \
    "v_pk_fma_f16 v102, v106, v126, v102\n\t" \
    "v_pk_fma_f16 v103, v107, v127, v103\n\t" \
    "s_waitcnt lgkmcnt(2)\n\t" \
    "v_pk_mul_f16 v92, v128, v92\n\t" \
    "v_pk_mul_f16 v93, v129, v93\n\t" \
    "v_mfma_f32_32x32x16_f16 v[32:47], v[112:115], v[100:103], v[32:47]\n\t" \
    "v_pk_mul_f16 v94, v130, v94\n\t" \
    "v_pk_mul_f16 v95, v131, v95\n\t" \
    "v_pk_fma_f16 v92, v104, v116, v92\n\t" \
    "v_pk_fma_f16 v93, v105, v117, v93\n\t" \
    "v_pk_fma_f16 v94, v106, v118, v94\n\t" \
    "v_pk_fma_f16 v95, v107, v119, v95\n\t" \
    "v_pk_mul_f16 v96, v128, v96\n\t" \
    "v_pk_mul_f16 v97, v129, v97\n\t" \
    "v_mfma_f32_32x32x16_f16 v[0:15], v[112:115], v[92:95], v[0:15]\n\t" \
    "v_pk_mul_f16 v98, v130, v98\n\t" \
    "v_pk_mul_f16 v99, v131, v99\n\t" \
    "v_pk_fma_f16 v96, v104, v120, v96\n\t" \
    "v_pk_fma_f16 v97, v105, v121, v97\n\t" \
    "v_pk_fma_f16 v98, v106, v122, v98\n\t" \
    "v_pk_fma_f16 v99, v107, v123, v99\n\t" \
    "s_waitcnt vmcnt(0) lgkmcnt(0)\n\t" \
    "s_nop 0\n\t" \
    "v_mfma_f32_32x32x16_f16 v[16:31], v[112:115], v[96:99], v[16:31]\n\t" \
    "v_lshrrev_b32 v139, 12, v136\n\t" \
    "v_and_b32 v139, 0xff0, v139\n\t" \
    "ds_read_b128 v[124:127], v144 offset:992\n\t" \
    "ds_read_b128 v[116:119], v144 offset:864\n\t" \
    "ds_read_b128 v[120:123], v144 offset:928\n\t" \
    "ds_read_b128 v[112:115], v139 offset:17408\n\t" \
    "s_barrier\n\t" \
    "ds_read_b64_tr_b16 v[104:105], v143 offset:0\n\t" \
    "ds_read_b64_tr_b16 v[106:107], v143 offset:2048\n\t" \
    "ds_read_b64_tr_b16 v[100:101], v142 offset:0\n\t" \
    "ds_read_b64_tr_b16 v[102:103], v142 offset:2048\n\t" \
    "ds_read_b64_tr_b16 v[92:93], v140 offset:0\n\t" \
    "ds_read_b64_tr_b16 v[94:95], v140 offset:2048\n\t" \
    "ds_read_b64_tr_b16 v[96:97], v141 offset:0\n\t" \
    "ds_read_b64_tr_b16 v[98:99], v141 offset:2048\n\t" \
    "v_and_b32 v147, 0x7fff, v137\n\t" \
    "v_add_u32 v147, v148, v147\n\t" \
    "ds_read_b128 v[88:91], v147\n\t" \
    "v_cvt_pk_f16_f32 v132, v72, v73\n\t" \
    "v_cvt_pk_f16_f32 v133, v74, v75\n\t" \
    "v_cvt_pk_f16_f32 v134, v76, v77\n\t" \
    "v_cvt_pk_f16_f32 v135, v78, v79\n\t" \
    "v_cvt_pk_f16_f32 v128, v56, v57\n\t" \
    "v_cvt_pk_f16_f32 v129, v58, v59\n\t" \
    "v_cvt_pk_f16_f32 v130, v60, v61\n\t" \
    "v_cvt_pk_f16_f32 v131, v62, v63\n\t" \
    "s_waitcnt lgkmcnt(5)\n\t" \
    "v_pk_mul_f16 v104, v132, v104\n\t" \
    "v_pk_mul_f16 v105, v133, v105\n\t" \
    "v_pk_mul_f16 v106, v134, v106\n\t" \
    "v_pk_mul_f16 v107, v135, v107\n\t" \
    "v_pk_mul_f16 v100, v128, v100\n\t" \
    "v_pk_mul_f16 v101, v129, v101\n\t" \
    "v_pk_mul_f16 v102, v130, v102\n\t" \
    "v_pk_mul_f16 v103, v131, v103\n\t" \
    "v_pk_fma_f16 v100, v104, v124, v100\n\t" \
    "v_pk_fma_f16 v101, v105, v125, v101\n\t" \
    "v_pk_fma_f16 v102, v106, v126, v102\n\t" \
    "v_pk_fma_f16 v103, v107, v127, v103\n\t" \
    "s_waitcnt lgkmcnt(1)\n\t" \
    "v_pk_mul_f16 v92, v128, v92\n\t" \
    "v_pk_mul_f16 v93, v129, v93\n\t" \
    "v_mfma_f32_32x32x16_f16 v[32:47], v[112:115], v[100:103], v[32:47]\n\t" \
    "v_pk_mul_f16 v94, v130, v94\n\t" \
    "v_pk_mul_f16 v95, v131, v95\n\t" \
    "v_pk_fma_f16 v92, v104, v116, v92\n\t" \
    "v_pk_fma_f16 v93, v105, v117, v93\n\t" \
    "v_pk_fma_f16 v94, v106, v118, v94\n\t" \
    "v_pk_fma_f16 v95, v107, v119, v95\n\t" \
    "v_pk_mul_f16 v96, v128, v96\n\t" \
    "v_pk_mul_f16 v97, v129, v97\n\t" \
    "v_mfma_f32_32x32x16_f16 v[0:15], v[112:115], v[92:95], v[0:15]\n\t" \
    "v_pk_mul_f16 v98, v130, v98\n\t" \
    "v_pk_mul_f16 v99, v131, v99\n\t" \
    "v_pk_fma_f16 v96, v104, v120, v96\n\t" \
    "v_pk_fma_f16 v97, v105, v121, v97\n\t" \
    "v_pk_fma_f16 v98, v106, v122, v98\n\t" \
    "v_pk_fma_f16 v99, v107, v123, v99\n\t" \
    "s_waitcnt vmcnt(0) lgkmcnt(0)\n\t" \
    "v_cmp_lt_u32 vcc, 0x7fff, v137\n\t" \
    "v_lshrrev_b32 v136, v149, v138\n\t" \
    "s_cbranch_vccnz LV_RARE_A1_%=\n\t" \
    "LV_BACK_A1_%=:\n\t" \
    "v_mfma_f32_32x32x16_f16 v[16:31], v[112:115], v[96:99], v[16:31]\n\t" \
    "v_and_b32 v139, 0xff, v136\n\t" \
    "v_lshlrev_b32 v139, 4, v139\n\t" \
    "ds_read_b128 v[124:127], v144 offset:1472\n\t" \
    "ds_read_b128 v[116:119], v144 offset:1344\n\t" \
    "ds_read_b128 v[120:123], v144 offset:1408\n\t" \
    "ds_read_b128 v[112:115], v139 offset:17408\n\t" \
    "s_barrier\n\t" \
    "s_add_i32 s46, s46, 1\n\t" \
    "s_cmp_eq_u32 s46, s47\n\t" \
    "s_cbranch_scc1 LV_END_%=\n\t" \
    "ds_read_b64_tr_b16 v[104:105], v143 offset:4096\n\t" \
    "ds_read_b64_tr_b16 v[106:107], v143 offset:6144\n\t" \
    "v_mfma_f32_32x32x16_bf16 v[64:79], v[88:91], v[84:87], 0\n\t" \
    "ds_read_b64_tr_b16 v[100:101], v142 offset:4096\n\t" \
    "ds_read_b64_tr_b16 v[102:103], v142 offset:6144\n\t" \
    "ds_read_b64_tr_b16 v[92:93], v140 offset:4096\n\t" \
    "ds_read_b64_tr_b16 v[94:95], v140 offset:6144\n\t" \
    "v_mfma_f32_32x32x16_bf16 v[48:63], v[88:91], v[80:83], 0\n\t" \
    "ds_read_b64_tr_b16 v[96:97], v141 offset:4096\n\t" \
    "ds_read_b64_tr_b16 v[98:99], v141 offset:6144\n\t" \
    "ds_read_u16 v137, v145 offset:128\n\t" \
    "ds_read_b32 v138, v146 offset:192\n\t" \
    "s_nop 3\n\t" \
    "v_cvt_pk_f16_f32 v132, v64, v65\n\t" \
    "v_cvt_pk_f16_f32 v133, v66, v67\n\t" \
    "v_cvt_pk_f16_f32 v134, v68, v69\n\t" \
    "v_cvt_pk_f16_f32 v135, v70, v71\n\t" \
    "v_cvt_pk_f16_f32 v128, v48, v49\n\t" \
    "v_cvt_pk_f16_f32 v129, v50, v51\n\t" \
    "v_cvt_pk_f16_f32 v130, v52, v53\n\t" \
    "v_cvt_pk_f16_f32 v131, v54, v55\n\t" \
    "s_waitcnt lgkmcnt(6)\n\t" \
    "v_pk_mul_f16 v104, v132, v104\n\t" \
    "v_pk_mul_f16 v105, v133, v105\n\t" \
    "v_pk_mul_f16 v106, v134, v106\n\t" \
    "v_pk_mul_f16 v107, v135, v107\n\t" \
    "v_pk_mul_f16 v100, v128, v100\n\t" \
    "v_pk_mul_f16 v101, v129, v101\n\t" \
    "v_pk_mul_f16 v102, v130, v102\n\t" \
    "v_pk_mul_f16 v103, v131, v103\n\t" \
    "v_pk_fma_f16 v100, v104, v124, v100\n\t" \
    "v_pk_fma_f16 v101, v105, v125, v101\n\t" \
    "v_pk_fma_f16 v102, v106, v126, v102\n\t" \
    "v_pk_fma_f16 v103, v107, v127, v103\n\t" \
    "s_waitcnt lgkmcnt(2)\n\t" \
    "v_pk_mul_f16 v92, v128, v92\n\t" \
    "v_pk_mul_f16 v93, v129, v93\n\t" \
    "v_mfma_f32_32x32x16_f16 v[32:47], v[112:115], v[100:103], v[32:47]\n\t" \
    "v_pk_mul_f16 v94, v130, v94\n\t" \
    "v_pk_mul_f16 v95, v131, v95\n\t" \
    "v_pk_fma_f16 v92, v104, v116, v92\n\t" \
    "v_pk_fma_f16 v93, v105, v117, v93\n\t" \
    "v_pk_fma_f16 v94, v106, v118, v94\n\t" \
    "v_pk_fma_f16 v95, v107, v119, v95\n\t" \
    "v_pk_mul_f16 v96, v128, v96\n\t" \
    "v_pk_mul_f16 v97, v129, v97\n\t" \
    "v_mfma_f32_32x32x16_f16 v[0:15], v[112:115], v[92:95], v[0:15]\n\t" \
    "v_pk_mul_f16 v98, v130, v98\n\t" \
    "v_pk_mul_f16 v99, v131, v99\n\t" \
    "v_pk_fma_f16 v96, v104, v120, v96\n\t" \
    "v_pk_fma_f16 v97, v105, v121, v97\n\t" \
    "v_pk_fma_f16 v98, v106, v122, v98\n\t" \
    "v_pk_fma_f16 v99, v107, v123, v99\n\t" \
    "s_waitcnt vmcnt(0) lgkmcnt(0)\n\t" \
    "s_nop 0\n\t" \
    "v_mfma_f32_32x32x16_f16 v[16:31], v[112:115], v[96:99], v[16:31]\n\t" \
    "v_lshrrev_b32 v139, 12, v136\n\t" \
    "v_and_b32 v139, 0xff0, v139\n\t" \
    "ds_read_b128 v[124:127], v144 offset:1504\n\t" \
    "ds_read_b128 v[116:119], v144 offset:1376\n\t" \
    "ds_read_b128 v[120:123], v144 offset:1440\n\t" \
    "ds_read_b128 v[112:115], v139 offset:17408\n\t" \
    "s_barrier\n\t" \
    "ds_read_b64_tr_b16 v[104:105], v143 offset:8192\n\t" \
    "ds_read_b64_tr_b16 v[106:107], v143 offset:10240\n\t" \
    "ds_read_b64_tr_b16 v[100:101], v142 offset:8192\n\t" \
    "ds_read_b64_tr_b16 v[102:103], v142 offset:10240\n\t" \
    "ds_read_b64_tr_b16 v[92:93], v140 offset:8192\n\t" \
    "ds_read_b64_tr_b16 v[94:95], v140 offset:10240\n\t" \
    "ds_read_b64_tr_b16 v[96:97], v141 offset:8192\n\t" \
    "ds_read_b64_tr_b16 v[98:99], v141 offset:10240\n\t" \
    "v_and_b32 v147, 0x7fff, v137\n\t" \
    "v_add_u32 v147, v148, v147\n\t" \
    "ds_read_b128 v[88:91], v147\n\t" \
    "v_cvt_pk_f16_f32 v132, v72, v73\n\t" \
    "v_cvt_pk_f16_f32 v133, v74, v75\n\t" \
    "v_cvt_pk_f16_f32 v134, v76, v77\n\t" \
    "v_cvt_pk_f16_f32 v135, v78, v79\n\t" \
    "v_cvt_pk_f16_f32 v128, v56, v57\n\t" \
    "v_cvt_pk_f16_f32 v129, v58, v59\n\t" \
    "v_cvt_pk_f16_f32 v130, v60, v61\n\t" \
    "v_cvt_pk_f16_f32 v131, v62, v63\n\t" \
    "s_waitcnt lgkmcnt(5)\n\t" \
    "v_pk_mul_f16 v104, v132, v104\n\t" \
    "v_pk_mul_f16 v105, v133, v105\n\t" \
    "v_pk_mul_f16 v106, v134, v106\n\t" \
    "v_pk_mul_f16 v107, v135, v107\n\t" \
    "v_pk_mul_f16 v100, v128, v100\n\t" \
    "v_pk_mul_f16 v101, v129, v101\n\t" \
    "v_pk_mul_f16 v102, v130, v102\n\t" \
    "v_pk_mul_f16 v103, v131, v103\n\t" \
    "v_pk_fma_f16 v100, v104, v124, v100\n\t" \
    "v_pk_fma_f16 v101, v105, v125, v101\n\t" \
    "v_pk_fma_f16 v102, v106, v126, v102\n\t" \
    "v_pk_fma_f16 v103, v107, v127, v103\n\t" \
    "s_waitcnt lgkmcnt(1)\n\t" \
    "v_pk_mul_f16 v92, v128, v92\n\t" \
    "v_pk_mul_f16 v93, v129, v93\n\t" \
    "v_mfma_f32_32x32x16_f16 v[32:47], v[112:115], v[100:103], v[32:47]\n\t" \
    "v_pk_mul_f16 v94, v130, v94\n\t" \
    "v_pk_mul_f16 v95, v131, v95\n\t" \
    "v_pk_fma_f16 v92, v104, v116, v92\n\t" \
    "v_pk_fma_f16 v93, v105, v117, v93\n\t" \
    "v_pk_fma_f16 v94, v106, v118, v94\n\t" \
    "v_pk_fma_f16 v95, v107, v119, v95\n\t" \
    "v_pk_mul_f16 v96, v128, v96\n\t" \
    "v_pk_mul_f16 v97, v129, v97\n\t" \
    "v_mfma_f32_32x32x16_f16 v[0:15], v[112:115], v[92:95], v[0:15]\n\t" \
    "v_pk_mul_f16 v98, v130, v98\n\t" \
    "v_pk_mul_f16 v99, v131, v99\n\t" \
    "v_pk_fma_f16 v96, v104, v120, v96\n\t" \
    "v_pk_fma_f16 v97, v105, v121, v97\n\t" \
    "v_pk_fma_f16 v98, v106, v122, v98\n\t" \
    "v_pk_fma_f16 v99, v107, v123, v99\n\t" \
    "s_waitcnt vmcnt(0) lgkmcnt(0)\n\t" \
    "v_cmp_lt_u32 vcc, 0x7fff, v137\n\t" \
    "v_lshrrev_b32 v136, v149, v138\n\t" \
    "s_cbranch_vccnz LV_RARE_A2_%=\n\t" \
    "LV_BACK_A2_%=:\n\t" \
    "v_mfma_f32_32x32x16_f16 v[16:31], v[112:115], v[96:99], v[16:31]\n\t" \
    "v_and_b32 v139, 0xff, v136\n\t" \
    "v_lshlrev_b32 v139, 4, v139\n\t" \
    "ds_read_b128 v[124:127], v144 offset:448\n\t" \
    "ds_read_b128 v[116:119], v144 offset:320\n\t" \
    "ds_read_b128 v[120:123], v144 offset:384\n\t" \
    "ds_read_b128 v[112:115], v139 offset:17408\n\t" \
    "s_barrier\n\t" \
    "s_add_i32 s46, s46, 1\n\t" \
    "s_cmp_eq_u32 s46, s47\n\t" \
    "s_cbranch_scc1 LV_END_%=\n\t" \
    "s_branch LA_LOOP_%=\n\t" \
    "LV_RARE_A0_%=:\n\t" \
    "v_and_b32 v147, 0x7fff, v137\n\t" \
    "v_lshlrev_b32 v147, 5, v147\n\t" \
    "v_cndmask_b32 v147, 0, v147, vcc\n\t" \
    "v_lshl_add_u32 v147, v149, 1, v147\n\t" \
    "global_load_dwordx4 v[128:131], v147, s[44:45]\n\t" \
    "s_waitcnt vmcnt(0) ; END\n\t" \
    "v_cndmask_b32 v88, v88, v128, vcc\n\t" \
    "v_cndmask_b32 v89, v89, v129, vcc\n\t" \
    "v_cndmask_b32 v90, v90, v130, vcc\n\t" \
    "v_cndmask_b32 v91, v91, v131, vcc\n\t" \
    "s_nop 1\n\t" \
    "s_branch LV_BACK_A0_%=\n\t" \
    "LV_RARE_A1_%=:\n\t" \
    "v_and_b32 v147, 0x7fff, v137\n\t" \
    "v_lshlrev_b32 v147, 5, v147\n\t" \
    "v_cndmask_b32 v147, 0, v147, vcc\n\t" \
    "v_lshl_add_u32 v147, v149, 1, v147\n\t" \
    "global_load_dwordx4 v[128:131], v147, s[44:45]\n\t" \
    "s_waitcnt vmcnt(0) ; END\n\t" \
    "v_cndmask_b32 v88, v88, v128, vcc\n\t" \
    "v_cndmask_b32 v89, v89, v129, vcc\n\t" \
    "v_cndmask_b32 v90, v90, v130, vcc\n\t" \
    "v_cndmask_b32 v91, v91, v131, vcc\n\t" \
    "s_nop 1\n\t" \
    "s_branch LV_BACK_A1_%=\n\t" \
    "LV_RARE_A2_%=:\n\t" \
    "v_and_b32 v147, 0x7fff, v137\n\t" \
    "v_lshlrev_b32 v147, 5, v147\n\t" \
    "v_cndmask_b32 v147, 0, v147, vcc\n\t" \
    "v_lshl_add_u32 v147, v149, 1, v147\n\t" \
    "global_load_dwordx4 v[128:131], v147, s[44:45]\n\t" \
    "s_waitcnt vmcnt(0) ; END\n\t" \
    "v_cndmask_b32 v88, v88, v128, vcc\n\t" \
    "v_cndmask_b32 v89, v89, v129, vcc\n\t" \
    "v_cndmask_b32 v90, v90, v130, vcc\n\t" \
    "v_cndmask_b32 v91, v91, v131, vcc\n\t" \
    "s_nop 1\n\t" \
    "s_branch LV_BACK_A2_%=\n\t" \
    "LB_LOOP_%=:\n\t" \
    "ds_read_b64_tr_b16 v[104:105], v143 offset:0\n\t" \
    "ds_read_b64_tr_b16 v[106:107], v143 offset:2048\n\t" \
    "v_mfma_f32_32x32x16_bf16 v[64:79], v[88:91], v[84:87], 0\n\t" \
    "ds_read_b64_tr_b16 v[100:101], v142 offset:0\n\t" \
    "ds_read_b64_tr_b16 v[102:103], v142 offset:2048\n\t" \
    "ds_read_b64_tr_b16 v[92:93], v140 offset:0\n\t" \
    "ds_read_b64_tr_b16 v[94:95], v140 offset:2048\n\t" \
    "v_mfma_f32_32x32x16_bf16 v[48:63], v[88:91], v[80:83], 0\n\t" \
    "ds_read_b64_tr_b16 v[96:97], v141 offset:0\n\t" \
    "ds_read_b64_tr_b16 v[98:99], v141 offset:2048\n\t" \
    "ds_read_u16 v137, v145 offset:640\n\t" \
    "ds_read_b32 v138, v146 offset:704\n\t" \
    "s_nop 3\n\t" \
    "v_cvt_pk_f16_f32 v132, v64, v65\n\t" \
    "v_cvt_pk_f16_f32 v133, v66, v67\n\t" \
    "v_cvt_pk_f16_f32 v134, v68, v69\n\t" \
    "v_cvt_pk_f16_f32 v135, v70, v71\n\t" \
    "v_cvt_pk_f16_f32 v128, v48, v49\n\t" \
    "v_cvt_pk_f16_f32 v129, v50, v51\n\t" \
    "v_cvt_pk_f16_f32 v130, v52, v53\n\t" \
    "v_cvt_pk_f16_f32 v131, v54, v55\n\t" \
    "s_waitcnt lgkmcnt(6)\n\t" \
    "v_pk_mul_f16 v104, v132, v104\n\t" \
    "v_pk_mul_f16 v105, v133, v105\n\t" \
    "v_pk_mul_f16 v106, v134, v106\n\t" \
    "v_pk_mul_f16 v107, v135, v107\n\t" \
    "v_pk_mul_f16 v100, v128, v100\n\t" \
    "v_pk_mul_f16 v101, v129, v101\n\t" \
    "v_pk_mul_f16 v102, v130, v102\n\t" \
    "v_pk_mul_f16 v103, v131, v103\n\t" \
    "v_pk_fma_f16 v100, v104, v124, v100\n\t" \
    "v_pk_fma_f16 v101, v105, v125, v101\n\t" \
    "v_pk_fma_f16 v102, v106, v126, v102\n\t" \
    "v_pk_fma_f16 v103, v107, v127, v103\n\t" \
    "s_waitcnt lgkmcnt(2)\n\t" \
    "s_barrier\n\t" \
    "v_pk_mul_f16 v92, v128, v92\n\t" \
    "v_pk_mul_f16 v93, v129, v93\n\t" \
    "v_mfma_f32_32x32x16_f16 v[32:47], v[112:115], v[100:103], v[32:47]\n\t" \
    "v_pk_mul_f16 v94, v130, v94\n\t" \
    "v_pk_mul_f16 v95, v131, v95\n\t" \
    "v_pk_fma_f16 v92, v104, v116, v92\n\t" \
    "v_pk_fma_f16 v93, v105, v117, v93\n\t" \
    "v_pk_fma_f16 v94, v106, v118, v94\n\t" \
    "v_pk_fma_f16 v95, v107, v119, v95\n\t" \
    "v_pk_mul_f16 v96, v128, v96\n\t" \
    "v_pk_mul_f16 v97, v129, v97\n\t" \
    "v_mfma_f32_32x32x16_f16 v[0:15], v[112:115], v[92:95], v[0:15]\n\t" \
    "v_pk_mul_f16 v98, v130, v98\n\t" \
    "v_pk_mul_f16 v99, v131, v99\n\t" \
    "v_pk_fma_f16 v96, v104, v120, v96\n\t" \
    "v_pk_fma_f16 v97, v105, v121, v97\n\t" \
    "v_pk_fma_f16 v98, v106, v122, v98\n\t" \
    "v_pk_fma_f16 v99, v107, v123, v99\n\t" \
    "s_waitcnt vmcnt(0) lgkmcnt(0)\n\t" \
    "s_nop 0\n\t" \
    "v_mfma_f32_32x32x16_f16 v[16:31], v[112:115], v[96:99], v[16:31]\n\t" \
    "v_lshrrev_b32 v139, 12, v136\n\t" \
    "v_and_b32 v139, 0xff0, v139\n\t" \
    "ds_read_b128 v[124:127], v144 offset:480\n\t" \
    "ds_read_b128 v[116:119], v144 offset:352\n\t" \
    "ds_read_b128 v[120:123], v144 offset:416\n\t" \
    "ds_read_b128 v[112:115], v139 offset:17408\n\t" \
    "ds_read_b64_tr_b16 v[104:105], v143 offset:4096\n\t" \
    "ds_read_b64_tr_b16 v[106:107], v143 offset:6144\n\t" \
    "ds_read_b64_tr_b16 v[100:101], v142 offset:4096\n\t" \
    "ds_read_b64_tr_b16 v[102:103], v142 offset:6144\n\t" \
    "ds_read_b64_tr_b16 v[92:93], v140 offset:4096\n\t" \
    "ds_read_b64_tr_b16 v[94:95], v140 offset:6144\n\t" \
    "ds_read_b64_tr_b16 v[96:97], v141 offset:4096\n\t" \
    "ds_read_b64_tr_b16 v[98:99], v141 offset:6144\n\t" \
    "v_and_b32 v147, 0x7fff, v137\n\t" \
    "v_add_u32 v147, v148, v147\n\t" \
    "ds_read_b128 v[88:91], v147\n\t" \
    "v_cvt_pk_f16_f32 v132, v72, v73\n\t" \
    "v_cvt_pk_f16_f32 v133, v74, v75\n\t" \
    "v_cvt_pk_f16_f32 v134, v76, v77\n\t" \
    "v_cvt_pk_f16_f32 v135, v78, v79\n\t" \
    "v_cvt_pk_f16_f32 v128, v56, v57\n\t" \
    "v_cvt_pk_f16_f32 v129, v58, v59\n\t" \
    "v_cvt_pk_f16_f32 v130, v60, v61\n\t" \
    "v_cvt_pk_f16_f32 v131, v62, v63\n\t" \
    "s_waitcnt lgkmcnt(5)\n\t" \
    "v_pk_mul_f16 v104, v132, v104\n\t" \
    "v_pk_mul_f16 v105, v133, v105\n\t" \
    "v_pk_mul_f16 v106, v134, v106\n\t" \
    "v_pk_mul_f16 v107, v135, v107\n\t" \
    "v_pk_mul_f16 v100, v128, v100\n\t" \
    "v_pk_mul_f16 v101, v129, v101\n\t" \
    "v_pk_mul_f16 v102, v130, v102\n\t" \
    "v_pk_mul_f16 v103, v131, v103\n\t" \
    "v_pk_fma_f16 v100, v104, v124, v100\n\t" \
    "v_pk_fma_f16 v101, v105, v125, v101\n\t" \
    "v_pk_fma_f16 v102, v106, v126, v102\n\t" \
    "v_pk_fma_f16 v103, v107, v127, v103\n\t" \
    "s_waitcnt lgkmcnt(1)\n\t" \
    "s_barrier\n\t" \
    "v_pk_mul_f16 v92, v128, v92\n\t" \
    "v_pk_mul_f16 v93, v129, v93\n\t" \
    "v_mfma_f32_32x32x16_f16 v[32:47], v[112:115], v[100:103], v[32:47]\n\t" \
    "v_pk_mul_f16 v94, v130, v94\n\t" \
    "v_pk_mul_f16 v95, v131, v95\n\t" \
    "v_pk_fma_f16 v92, v104, v116, v92\n\t" \
    "v_pk_fma_f16 v93, v105, v117, v93\n\t" \
    "v_pk_fma_f16 v94, v106, v118, v94\n\t" \
    "v_pk_fma_f16 v95, v107, v119, v95\n\t" \
    "v_pk_mul_f16 v96, v128, v96\n\t" \
    "v_pk_mul_f16 v97, v129, v97\n\t" \
    "v_mfma_f32_32x32x16_f16 v[0:15], v[112:115], v[92:95], v[0:15]\n\t" \
    "v_pk_mul_f16 v98, v130, v98\n\t" \
    "v_pk_mul_f16 v99, v131, v99\n\t" \
    "v_pk_fma_f16 v96, v104, v120, v96\n\t" \
    "v_pk_fma_f16 v97, v105, v121, v97\n\t" \
    "v_pk_fma_f16 v98, v106, v122, v98\n\t" \
    "v_pk_fma_f16 v99, v107, v123, v99\n\t" \
    "s_waitcnt vmcnt(0) lgkmcnt(0)\n\t" \
    "v_cmp_lt_u32 vcc, 0x7fff, v137\n\t" \
    "v_lshrrev_b32 v136, v149, v138\n\t" \
    "s_cbranch_vccnz LV_RARE_B0_%=\n\t" \
    "LV_BACK_B0_%=:\n\t" \
    "v_mfma_f32_32x32x16_f16 v[16:31], v[112:115], v[96:99], v[16:31]\n\t" \
    "v_and_b32 v139, 0xff, v136\n\t" \
    "v_lshlrev_b32 v139, 4, v139\n\t" \
    "ds_read_b128 v[124:127], v144 offset:960\n\t" \
    "ds_read_b128 v[116:119], v144 offset:832\n\t" \
    "ds_read_b128 v[120:123], v144 offset:896\n\t" \
    "ds_read_b128 v[112:115], v139 offset:17408\n\t" \
    "s_add_i32 s46, s46, 1\n\t" \
    "s_cmp_eq_u32 s46, s47\n\t" \
    "s_cbranch_scc1 LV_END_%=\n\t" \
    "ds_read_b64_tr_b16 v[104:105], v143 offset:8192\n\t" \
    "ds_read_b64_tr_b16 v[106:107], v143 offset:10240\n\t" \
    "v_mfma_f32_32x32x16_bf16 v[64:79], v[88:91], v[84:87], 0\n\t" \
    "ds_read_b64_tr_b16 v[100:101], v142 offset:8192\n\t" \
    "ds_read_b64_tr_b16 v[102:103], v142 offset:10240\n\t" \
    "ds_read_b64_tr_b16 v[92:93], v140 offset:8192\n\t" \
    "ds_read_b64_tr_b16 v[94:95], v140 offset:10240\n\t" \
    "v_mfma_f32_32x32x16_bf16 v[48:63], v[88:91], v[80:83], 0\n\t" \
    "ds_read_b64_tr_b16 v[96:97], v141 offset:8192\n\t" \
    "ds_read_b64_tr_b16 v[98:99], v141 offset:10240\n\t" \
    "ds_read_u16 v137, v145 offset:1152\n\t" \
    "ds_read_b32 v138, v146 offset:1216\n\t" \
    "s_nop 3\n\t" \
    "v_cvt_pk_f16_f32 v132, v64, v65\n\t" \
    "v_cvt_pk_f16_f32 v133, v66, v67\n\t" \
    "v_cvt_pk_f16_f32 v134, v68, v69\n\t" \
    "v_cvt_pk_f16_f32 v135, v70, v71\n\t" \
    "v_cvt_pk_f16_f32 v128, v48, v49\n\t" \
    "v_cvt_pk_f16_f32 v129, v50, v51\n\t" \
    "v_cvt_pk_f16_f32 v130, v52, v53\n\t" \
    "v_cvt_pk_f16_f32 v131, v54, v55\n\t" \
    "s_waitcnt lgkmcnt(6)\n\t" \
    "v_pk_mul_f16 v104, v132, v104\n\t" \
    "v_pk_mul_f16 v105, v133, v105\n\t" \
    "v_pk_mul_f16 v106, v134, v106\n\t" \
    "v_pk_mul_f16 v107, v135, v107\n\t" \
    "v_pk_mul_f16 v100, v128, v100\n\t" \
    "v_pk_mul_f16 v101, v129, v101\n\t" \
    "v_pk_mul_f16 v102, v130, v102\n\t" \
    "v_pk_mul_f16 v103, v131, v103\n\t" \
    "v_pk_fma_f16 v100, v104, v124, v100\n\t" \
    "v_pk_fma_f16 v101, v105, v125, v101\n\t" \
    "v_pk_fma_f16 v102, v106, v126, v102\n\t" \
    "v_pk_fma_f16 v103, v107, v127, v103\n\t" \
    "s_waitcnt lgkmcnt(2)\n\t" \
    "s_barrier\n\t" \
    "v_pk_mul_f16 v92, v128, v92\n\t" \
    "v_pk_mul_f16 v93, v129, v93\n\t" \
    "v_mfma_f32_32x32x16_f16 v[32:47], v[112:115], v[100:103], v[32:47]\n\t" \
    "v_pk_mul_f16 v94, v130, v94\n\t" \
    "v_pk_mul_f16 v95, v131, v95\n\t" \
    "v_pk_fma_f16 v92, v104, v116, v92\n\t" \
    "v_pk_fma_f16 v93, v105, v117, v93\n\t" \
    "v_pk_fma_f16 v94, v106, v118, v94\n\t" \
    "v_pk_fma_f16 v95, v107, v119, v95\n\t" \
    "v_pk_mul_f16 v96, v128, v96\n\t" \
    "v_pk_mul_f16 v97, v129, v97\n\t" \
    "v_mfma_f32_32x32x16_f16 v[0:15], v[112:115], v[92:95], v[0:15]\n\t" \
    "v_pk_mul_f16 v98, v130, v98\n\t" \
    "v_pk_mul_f16 v99, v131, v99\n\t" \
    "v_pk_fma_f16 v96, v104, v120, v96\n\t" \
    "v_pk_fma_f16 v97, v105, v121, v97\n\t" \
    "v_pk_fma_f16 v98, v106, v122, v98\n\t" \
    "v_pk_fma_f16 v99, v107, v123, v99\n\t" \
    "s_waitcnt vmcnt(0) lgkmcnt(0)\n\t" \
    "s_nop 0\n\t" \
    "v_mfma_f32_32x32x16_f16 v[16:31], v[112:115], v[96:99], v[16:31]\n\t" \
    "v_lshrrev_b32 v139, 12, v136\n\t" \
    "v_and_b32 v139, 0xff0, v139\n\t" \
    "ds_read_b128 v[124:127], v144 offset:992\n\t" \
    "ds_read_b128 v[116:119], v144 offset:864\n\t" \
    "ds_read_b128 v[120:123], v144 offset:928\n\t" \
    "ds_read_b128 v[112:115], v139 offset:17408\n\t" \
    "ds_read_b64_tr_b16 v[104:105], v143 offset:0\n\t" \
    "ds_read_b64_tr_b16 v[106:107], v143 offset:2048\n\t" \
    "ds_read_b64_tr_b16 v[100:101], v142 offset:0\n\t" \
    "ds_read_b64_tr_b16 v[102:103], v142 offset:2048\n\t" \
    "ds_read_b64_tr_b16 v[92:93], v140 offset:0\n\t" \
    "ds_read_b64_tr_b16 v[94:95], v140 offset:2048\n\t" \
    "ds_read_b64_tr_b16 v[96:97], v141 offset:0\n\t" \
    "ds_read_b64_tr_b16 v[98:99], v141 offset:2048\n\t" \
    "v_and_b32 v147, 0x7fff, v137\n\t" \
    "v_add_u32 v147, v148, v147\n\t" \
    "ds_read_b128 v[88:91], v147\n\t" \
    "v_cvt_pk_f16_f32 v132, v72, v73\n\t" \
    "v_cvt_pk_f16_f32 v133, v74, v75\n\t" \
    "v_cvt_pk_f16_f32 v134, v76, v77\n\t" \
    "v_cvt_pk_f16_f32 v135, v78, v79\n\t" \
    "v_cvt_pk_f16_f32 v128, v56, v57\n\t" \
    "v_cvt_pk_f16_f32 v129, v58, v59\n\t" \
    "v_cvt_pk_f16_f32 v130, v60, v61\n\t" \
    "v_cvt_pk_f16_f32 v131, v62, v63\n\t" \
    "s_waitcnt lgkmcnt(5)\n\t" \
    "v_pk_mul_f16 v104, v132, v104\n\t" \
    "v_pk_mul_f16 v105, v133, v105\n\t" \
    "v_pk_mul_f16 v106, v134, v106\n\t" \
    "v_pk_mul_f16 v107, v135, v107\n\t" \
    "v_pk_mul_f16 v100, v128, v100\n\t" \
    "v_pk_mul_f16 v101, v129, v101\n\t" \
    "v_pk_mul_f16 v102, v130, v102\n\t" \
    "v_pk_mul_f16 v103, v131, v103\n\t" \
    "v_pk_fma_f16 v100, v104, v124, v100\n\t" \
    "v_pk_fma_f16 v101, v105, v125, v101\n\t" \
    "v_pk_fma_f16 v102, v106, v126, v102\n\t" \
    "v_pk_fma_f16 v103, v107, v127, v103\n\t" \
    "s_waitcnt lgkmcnt(1)\n\t" \
    "s_barrier\n\t" \
    "v_pk_mul_f16 v92, v128, v92\n\t" \
    "v_pk_mul_f16 v93, v129, v93\n\t" \
    "v_mfma_f32_32x32x16_f16 v[32:47], v[112:115], v[100:103], v[32:47]\n\t" \
    "v_pk_mul_f16 v94, v130, v94\n\t" \
    "v_pk_mul_f16 v95, v131, v95\n\t" \
    "v_pk_fma_f16 v92, v104, v116, v92\n\t" \
    "v_pk_fma_f16 v93, v105, v117, v93\n\t" \
    "v_pk_fma_f16 v94, v106, v118, v94\n\t" \
    "v_pk_fma_f16 v95, v107, v119, v95\n\t" \
    "v_pk_mul_f16 v96, v128, v96\n\t" \
    "v_pk_mul_f16 v97, v129, v97\n\t" \
    "v_mfma_f32_32x32x16_f16 v[0:15], v[112:115], v[92:95], v[0:15]\n\t" \
    "v_pk_mul_f16 v98, v130, v98\n\t" \
    "v_pk_mul_f16 v99, v131, v99\n\t" \
    "v_pk_fma_f16 v96, v104, v120, v96\n\t" \
    "v_pk_fma_f16 v97, v105, v121, v97\n\t" \
    "v_pk_fma_f16 v98, v106, v122, v98\n\t" \
    "v_pk_fma_f16 v99, v107, v123, v99\n\t" \
    "s_waitcnt vmcnt(0) lgkmcnt(0)\n\t" \
    "v_cmp_lt_u32 vcc, 0x7fff, v137\n\t" \
    "v_lshrrev_b32 v136, v149, v138\n\t" \
    "s_cbranch_vccnz LV_RARE_B1_%=\n\t" \
    "LV_BACK_B1_%=:\n\t" \
    "v_mfma_f32_32x32x16_f16 v[16:31], v[112:115], v[96:99], v[16:31]\n\t" \
    "v_and_b32 v139, 0xff, v136\n\t" \
    "v_lshlrev_b32 v139, 4, v139\n\t" \
    "ds_read_b128 v[124:127], v144 offset:1472\n\t" \
    "ds_read_b128 v[116:119], v144 offset:1344\n\t" \
    "ds_read_b128 v[120:123], v144 offset:1408\n\t" \
    "ds_read_b128 v[112:115], v139 offset:17408\n\t" \
    "s_add_i32 s46, s46, 1\n\t" \
    "s_cmp_eq_u32 s46, s47\n\t" \
    "s_cbranch_scc1 LV_END_%=\n\t" \
    "ds_read_b64_tr_b16 v[104:105], v143 offset:4096\n\t" \
    "ds_read_b64_tr_b16 v[106:107], v143 offset:6144\n\t" \
    "v_mfma_f32_32x32x16_bf16 v[64:79], v[88:91], v[84:87], 0\n\t" \
    "ds_read_b64_tr_b16 v[100:101], v142 offset:4096\n\t" \
    "ds_read_b64_tr_b16 v[102:103], v142 offset:6144\n\t" \
    "ds_read_b64_tr_b16 v[92:93], v140 offset:4096\n\t" \
    "ds_read_b64_tr_b16 v[94:95], v140 offset:6144\n\t" \
    "v_mfma_f32_32x32x16_bf16 v[48:63], v[88:91], v[80:83], 0\n\t" \
    "ds_read_b64_tr_b16 v[96:97], v141 offset:4096\n\t" \
    "ds_read_b64_tr_b16 v[98:99], v141 offset:6144\n\t" \
    "ds_read_u16 v137, v145 offset:128\n\t" \
    "ds_read_b32 v138, v146 offset:192\n\t" \
    "s_nop 3\n\t" \
    "v_cvt_pk_f16_f32 v132, v64, v65\n\t" \
    "v_cvt_pk_f16_f32 v133, v66, v67\n\t" \
    "v_cvt_pk_f16_f32 v134, v68, v69\n\t" \
    "v_cvt_pk_f16_f32 v135, v70, v71\n\t" \
    "v_cvt_pk_f16_f32 v128, v48, v49\n\t" \
    "v_cvt_pk_f16_f32 v129, v50, v51\n\t" \
    "v_cvt_pk_f16_f32 v130, v52, v53\n\t" \
    "v_cvt_pk_f16_f32 v131, v54, v55\n\t" \
    "s_waitcnt lgkmcnt(6)\n\t" \
    "v_pk_mul_f16 v104, v132, v104\n\t" \
    "v_pk_mul_f16 v105, v133, v105\n\t" \
    "v_pk_mul_f16 v106, v134, v106\n\t" \
    "v_pk_mul_f16 v107, v135, v107\n\t" \
    "v_pk_mul_f16 v100, v128, v100\n\t" \
    "v_pk_mul_f16 v101, v129, v101\n\t" \
    "v_pk_mul_f16 v102, v130, v102\n\t" \
    "v_pk_mul_f16 v103, v131, v103\n\t" \
    "v_pk_fma_f16 v100, v104, v124, v100\n\t" \
    "v_pk_fma_f16 v101, v105, v125, v101\n\t" \
    "v_pk_fma_f16 v102, v106, v126, v102\n\t" \
    "v_pk_fma_f16 v103, v107, v127, v103\n\t" \
    "s_waitcnt lgkmcnt(2)\n\t" \
    "s_barrier\n\t" \
    "v_pk_mul_f16 v92, v128, v92\n\t" \
    "v_pk_mul_f16 v93, v129, v93\n\t" \
    "v_mfma_f32_32x32x16_f16 v[32:47], v[112:115], v[100:103], v[32:47]\n\t" \
    "v_pk_mul_f16 v94, v130, v94\n\t" \
    "v_pk_mul_f16 v95, v131, v95\n\t" \
    "v_pk_fma_f16 v92, v104, v116, v92\n\t" \
    "v_pk_fma_f16 v93, v105, v117, v93\n\t" \
    "v_pk_fma_f16 v94, v106, v118, v94\n\t" \
    "v_pk_fma_f16 v95, v107, v119, v95\n\t" \
    "v_pk_mul_f16 v96, v128, v96\n\t" \
    "v_pk_mul_f16 v97, v129, v97\n\t" \
    "v_mfma_f32_32x32x16_f16 v[0:15], v[112:115], v[92:95], v[0:15]\n\t" \
    "v_pk_mul_f16 v98, v130, v98\n\t" \
    "v_pk_mul_f16 v99, v131, v99\n\t" \
    "v_pk_fma_f16 v96, v104, v120, v96\n\t" \
    "v_pk_fma_f16 v97, v105, v121, v97\n\t" \
    "v_pk_fma_f16 v98, v106, v122, v98\n\t" \
    "v_pk_fma_f16 v99, v107, v123, v99\n\t" \
    "s_waitcnt vmcnt(0) lgkmcnt(0)\n\t" \
    "s_nop 0\n\t" \
    "v_mfma_f32_32x32x16_f16 v[16:31], v[112:115], v[96:99], v[16:31]\n\t" \
    "v_lshrrev_b32 v139, 12, v136\n\t" \
    "v_and_b32 v139, 0xff0, v139\n\t" \
    "ds_read_b128 v[124:127], v144 offset:1504\n\t" \
    "ds_read_b128 v[116:119], v144 offset:1376\n\t" \
    "ds_read_b128 v[120:123], v144 offset:1440\n\t" \
    "ds_read_b128 v[112:115], v139 offset:17408\n\t" \
    "ds_read_b64_tr_b16 v[104:105], v143 offset:8192\n\t" \
    "ds_read_b64_tr_b16 v[106:107], v143 offset:10240\n\t" \
    "ds_read_b64_tr_b16 v[100:101], v142 offset:8192\n\t" \
    "ds_read_b64_tr_b16 v[102:103], v142 offset:10240\n\t" \
    "ds_read_b64_tr_b16 v[92:93], v140 offset:8192\n\t" \
    "ds_read_b64_tr_b16 v[94:95], v140 offset:10240\n\t" \
    "ds_read_b64_tr_b16 v[96:97], v141 offset:8192\n\t" \
    "ds_read_b64_tr_b16 v[98:99], v141 offset:10240\n\t" \
    "v_and_b32 v147, 0x7fff, v137\n\t" \
    "v_add_u32 v147, v148, v147\n\t" \
    "ds_read_b128 v[88:91], v147\n\t" \
    "v_cvt_pk_f16_f32 v132, v72, v73\n\t" \
    "v_cvt_pk_f16_f32 v133, v74, v75\n\t" \
    "v_cvt_pk_f16_f32 v134, v76, v77\n\t" \
    "v_cvt_pk_f16_f32 v135, v78, v79\n\t" \
    "v_cvt_pk_f16_f32 v128, v56, v57\n\t" \
    "v_cvt_pk_f16_f32 v129, v58, v59\n\t" \
    "v_cvt_pk_f16_f32 v130, v60, v61\n\t" \
    "v_cvt_pk_f16_f32 v131, v62, v63\n\t" \
    "s_waitcnt lgkmcnt(5)\n\t" \
    "v_pk_mul_f16 v104, v132, v104\n\t" \
    "v_pk_mul_f16 v105, v133, v105\n\t" \
    "v_pk_mul_f16 v106, v134, v106\n\t" \
    "v_pk_mul_f16 v107, v135, v107\n\t" \
    "v_pk_mul_f16 v100, v128, v100\n\t" \
    "v_pk_mul_f16 v101, v129, v101\n\t" \
    "v_pk_mul_f16 v102, v130, v102\n\t" \
    "v_pk_mul_f16 v103, v131, v103\n\t" \
    "v_pk_fma_f16 v100, v104, v124, v100\n\t" \
    "v_pk_fma_f16 v101, v105, v125, v101\n\t" \
    "v_pk_fma_f16 v102, v106, v126, v102\n\t" \
    "v_pk_fma_f16 v103, v107, v127, v103\n\t" \
    "s_waitcnt lgkmcnt(1)\n\t" \
    "s_barrier\n\t" \
    "v_pk_mul_f16 v92, v128, v92\n\t" \
    "v_pk_mul_f16 v93, v129, v93\n\t" \
    "v_mfma_f32_32x32x16_f16 v[32:47], v[112:115], v[100:103], v[32:47]\n\t" \
    "v_pk_mul_f16 v94, v130, v94\n\t" \
    "v_pk_mul_f16 v95, v131, v95\n\t" \
    "v_pk_fma_f16 v92, v104, v116, v92\n\t" \
    "v_pk_fma_f16 v93, v105, v117, v93\n\t" \
    "v_pk_fma_f16 v94, v106, v118, v94\n\t" \
    "v_pk_fma_f16 v95, v107, v119, v95\n\t" \
    "v_pk_mul_f16 v96, v128, v96\n\t" \
    "v_pk_mul_f16 v97, v129, v97\n\t" \
    "v_mfma_f32_32x32x16_f16 v[0:15], v[112:115], v[92:95], v[0:15]\n\t" \
    "v_pk_mul_f16 v98, v130, v98\n\t" \
    "v_pk_mul_f16 v99, v131, v99\n\t" \
    "v_pk_fma_f16 v96, v104, v120, v96\n\t" \
    "v_pk_fma_f16 v97, v105, v121, v97\n\t" \
    "v_pk_fma_f16 v98, v106, v122, v98\n\t" \
    "v_pk_fma_f16 v99, v107, v123, v99\n\t" \
    "s_waitcnt vmcnt(0) lgkmcnt(0)\n\t" \
    "v_cmp_lt_u32 vcc, 0x7fff, v137\n\t" \
    "v_lshrrev_b32 v136, v149, v138\n\t" \
    "s_cbranch_vccnz LV_RARE_B2_%=\n\t" \
    "LV_BACK_B2_%=:\n\t" \
    "v_mfma_f32_32x32x16_f16 v[16:31], v[112:115], v[96:99], v[16:31]\n\t" \
    "v_and_b32 v139, 0xff, v136\n\t" \
    "v_lshlrev_b32 v139, 4, v139\n\t" \
    "ds_read_b128 v[124:127], v144 offset:448\n\t" \
    "ds_read_b128 v[116:119], v144 offset:320\n\t" \
    "ds_read_b128 v[120:123], v144 offset:384\n\t" \
    "ds_read_b128 v[112:115], v139 offset:17408\n\t" \
    "s_add_i32 s46, s46, 1\n\t" \
    "s_cmp_eq_u32 s46, s47\n\t" \
    "s_cbranch_scc1 LV_END_%=\n\t" \
    "s_branch LB_LOOP_%=\n\t" \
    "LV_RARE_B0_%=:\n\t" \
    "v_and_b32 v147, 0x7fff, v137\n\t" \
    "v_lshlrev_b32 v147, 5, v147\n\t" \
    "v_cndmask_b32 v147, 0, v147, vcc\n\t" \
    "v_lshl_add_u32 v147, v149, 1, v147\n\t" \
    "global_load_dwordx4 v[128:131], v147, s[44:45]\n\t" \
    "s_waitcnt vmcnt(0) ; END\n\t" \
    "v_cndmask_b32 v88, v88, v128, vcc\n\t" \
    "v_cndmask_b32 v89, v89, v129, vcc\n\t" \
    "v_cndmask_b32 v90, v90, v130, vcc\n\t" \
    "v_cndmask_b32 v91, v91, v131, vcc\n\t" \
    "s_nop 1\n\t" \
    "s_branch LV_BACK_B0_%=\n\t" \
    "LV_RARE_B1_%=:\n\t" \
    "v_and_b32 v147, 0x7fff, v137\n\t" \
    "v_lshlrev_b32 v147, 5, v147\n\t" \
    "v_cndmask_b32 v147, 0, v147, vcc\n\t" \
    "v_lshl_add_u32 v147, v149, 1, v147\n\t" \
    "global_load_dwordx4 v[128:131], v147, s[44:45]\n\t" \
    "s_waitcnt vmcnt(0) ; END\n\t" \
    "v_cndmask_b32 v88, v88, v128, vcc\n\t" \
    "v_cndmask_b32 v89, v89, v129, vcc\n\t" \
    "v_cndmask_b32 v90, v90, v130, vcc\n\t" \
    "v_cndmask_b32 v91, v91, v131, vcc\n\t" \
    "s_nop 1\n\t" \
    "s_branch LV_BACK_B1_%=\n\t" \
    "LV_RARE_B2_%=:\n\t" \
    "v_and_b32 v147, 0x7fff, v137\n\t" \
    "v_lshlrev_b32 v147, 5, v147\n\t" \
    "v_cndmask_b32 v147, 0, v147, vcc\n\t" \
    "v_lshl_add_u32 v147, v149, 1, v147\n\t" \
    "global_load_dwordx4 v[128:131], v147, s[44:45]\n\t" \
    "s_waitcnt vmcnt(0) ; END\n\t" \
    "v_cndmask_b32 v88, v88, v128, vcc\n\t" \
    "v_cndmask_b32 v89, v89, v129, vcc\n\t" \
    "v_cndmask_b32 v90, v90, v130, vcc\n\t" \
    "v_cndmask_b32 v91, v91, v131, vcc\n\t" \
    "s_nop 1\n\t" \
    "s_branch LV_BACK_B2_%=\n\t" \
    "LV_END_%=:\n\t" \
    "s_waitcnt vmcnt(0) lgkmcnt(0) ; END\n\t" \
    "s_barrier ; END\n\t" \
    "s_nop 7\n\t" \
    "s_nop 7\n\t"
#define K2_DBG 0
#define K2_V_CLOBBERS "v48", "v49", "v50", "v51", "v52", "v53", "v54", "v55", "v56", "v57", "v58", "v59", "v60", "v61", "v62", "v63", "v64", "v65", "v66", "v67", "v68", "v69", "v70", "v71", "v72", "v73", "v74", "v75", "v76", "v77", "v78", "v79", "v92", "v93", "v94", "v95", "v96", "v97", "v98", "v99", "v100", "v101", "v102", "v103", "v104", "v105", "v106", "v107", "v108", "v109", "v110", "v111", "v112", "v113", "v114", "v115", "v116", "v117", "v118", "v119", "v120", "v121", "v122", "v123", "v124", "v125", "v126", "v127", "v128", "v129", "v130", "v131", "v132", "v133", "v134", "v135", "v137", "v138", "v139", "v147", "vcc", "scc", "memory"
#define K2_S_LOOP_TEXT \
    "s_nop 1\n\t" \
    "ds_read_b128 v[100:103], v125 offset:512\n\t" \
    "ds_read_b128 v[104:107], v126 offset:512\n\t" \
    "ds_read_b32 v108, v117 offset:512\n\t" \
    "ds_read_b32 v109, v117 offset:520\n\t" \
    "v_and_b32 v113, 0xff, v110\n\t" \
    "v_lshlrev_b32 v113, 4, v113\n\t" \
    "ds_read_b128 v[88:91], v113 offset:17408\n\t" \
    "s_waitcnt lgkmcnt(1)\n\t" \
    "LS_LOOP_%=:\n\t" \
    "s_add_i32 m0, s58, 1024\n\t" \
    "s_mov_b64 exec, s[52:53]\n\t" \
    "global_load_lds_dwordx4 v124, s[48:49]\n\t" \
    "s_mov_b64 exec, -1\n\t" \
    "s_add_i32 m0, s59, 1024\n\t" \
    "s_mov_b64 exec, s[52:53]\n\t" \
    "global_load_lds_dwordx4 v124, s[50:51]\n\t" \
    "s_mov_b64 exec, -1\n\t" \
    "s_add_i32 m0, s56, 8192\n\t" \
    "v_add_u32 v100, v127, v100\n\t" \
    "global_load_lds_dwordx4 v100, s[40:41]\n\t" \
    "ds_read_b64_tr_b16 v[76:77], v114 offset:0\n\t" \
    "ds_read_b64_tr_b16 v[78:79], v114 offset:1024\n\t" \
    "ds_read_b64_tr_b16 v[80:81], v115 offset:0\n\t" \
    "ds_read_b64_tr_b16 v[82:83], v115 offset:1024\n\t" \
    "v_mfma_f32_32x32x16_bf16 v[32:47], v[72:75], v[64:67], 0\n\t" \
    "s_add_i32 m0, s57, 8192\n\t" \
    "v_add_u32 v104, v128, v104\n\t" \
    "global_load_lds_dwordx4 v104, s[40:41]\n\t" \
    "ds_read_u16 v111, v118 offset:640\n\t" \
    "ds_read_b32 v112, v119 offset:704\n\t" \
    "v_mfma_f32_32x32x16_bf16 v[48:63], v[72:75], v[68:71], 0\n\t" \
    "s_add_i32 m0, s56, 9216\n\t" \
    "v_add_u32 v101, v127, v101\n\t" \
    "global_load_lds_dwordx4 v101, s[40:41]\n\t" \
    "s_add_i32 m0, s57, 9216\n\t" \
    "v_add_u32 v105, v128, v105\n\t" \
    "global_load_lds_dwordx4 v105, s[40:41]\n\t" \
    "ds_read_b128 v[130:133], v125 offset:528\n\t" \
    "ds_read_b128 v[134:137], v126 offset:528\n\t" \
    "ds_read_b32 v138, v117 offset:528\n\t" \
    "ds_read_b32 v139, v117 offset:536\n\t" \
    "s_add_i32 s61, s46, 2\n\t" \
    "s_cmp_lt_u32 s61, s54\n\t" \
    "s_cselect_b32 s61, 512, 0\n\t" \
    "s_add_u32 s48, s48, s61\n\t" \
    "s_addc_u32 s49, s49, 0\n\t" \
    "s_add_i32 s61, s46, 2\n\t" \
    "s_cmp_lt_u32 s61, s55\n\t" \
    "s_cselect_b32 s61, 512, 0\n\t" \
    "s_add_u32 s50, s50, s61\n\t" \
    "s_addc_u32 s51, s51, 0\n\t" \
    "v_cvt_pk_f16_f32 v92, v32, v33\n\t" \
    "v_cvt_pk_f16_f32 v93, v34, v35\n\t" \
    "v_cvt_pk_f16_f32 v94, v36, v37\n\t" \
    "v_cvt_pk_f16_f32 v95, v38, v39\n\t" \
    "s_add_i32 m0, s56, 10240\n\t" \
    "v_add_u32 v102, v127, v102\n\t" \
    "global_load_lds_dwordx4 v102, s[40:41]\n\t" \
    "s_waitcnt lgkmcnt(4)\n\t" \
    "v_pk_mul_f16 v76, v92, v76\n\t" \
    "v_pk_mul_f16 v77, v93, v77\n\t" \
    "v_pk_mul_f16 v78, v94, v78\n\t" \
    "v_pk_mul_f16 v79, v95, v79\n\t" \
    "s_add_i32 m0, s57, 10240\n\t" \
    "v_add_u32 v106, v128, v106\n\t" \
    "global_load_lds_dwordx4 v106, s[40:41]\n\t" \
    "v_cvt_pk_f16_f32 v96, v48, v49\n\t" \
    "v_cvt_pk_f16_f32 v97, v50, v51\n\t" \
    "v_mfma_f32_32x32x16_f16 v[0:15], v[88:91], v[76:79], v[0:15]\n\t" \
    "v_cvt_pk_f16_f32 v98, v52, v53\n\t" \
    "v_cvt_pk_f16_f32 v99, v54, v55\n\t" \
    "s_add_i32 m0, s60, 0\n\t" \
    "v_add_u32 v108, v122, v108\n\t" \
    "global_load_lds_dwordx4 v108, s[40:41]\n\t" \
    "v_pk_mul_f16 v80, v96, v80\n\t" \
    "v_pk_mul_f16 v81, v97, v81\n\t" \
    "v_pk_mul_f16 v82, v98, v82\n\t" \
    "v_pk_mul_f16 v83, v99, v83\n\t" \
    "s_add_i32 m0, s56, 11264\n\t" \
    "v_add_u32 v103, v127, v103\n\t" \
    "global_load_lds_dwordx4 v103, s[40:41]\n\t" \
    "v_mfma_f32_32x32x16_f16 v[16:31], v[88:91], v[80:83], v[16:31]\n\t" \
    "s_add_i32 m0, s60, 1024\n\t" \
    "v_add_u32 v109, v122, v109\n\t" \
    "global_load_lds_dwordx4 v109, s[40:41]\n\t" \
    "v_lshrrev_b32 v113, 12, v110\n\t" \
    "v_and_b32 v113, 0xff0, v113\n\t" \
    "ds_read_b128 v[88:91], v113 offset:17408\n\t" \
    "s_add_i32 m0, s57, 11264\n\t" \
    "v_add_u32 v107, v128, v107\n\t" \
    "global_load_lds_dwordx4 v107, s[40:41]\n\t" \
    "s_waitcnt vmcnt(12) lgkmcnt(1)\n\t" \
    "s_barrier\n\t" \
    "s_add_i32 m0, s56, 0\n\t" \
    "v_add_u32 v130, v127, v130\n\t" \
    "global_load_lds_dwordx4 v130, s[40:41]\n\t" \
    "ds_read_b64_tr_b16 v[76:77], v114 offset:2048\n\t" \
    "ds_read_b64_tr_b16 v[78:79], v114 offset:3072\n\t" \
    "ds_read_b64_tr_b16 v[80:81], v115 offset:2048\n\t" \
    "ds_read_b64_tr_b16 v[82:83], v115 offset:3072\n\t" \
    "s_add_i32 m0, s57, 0\n\t" \
    "v_add_u32 v134, v128, v134\n\t" \
    "global_load_lds_dwordx4 v134, s[40:41]\n\t" \
    "v_and_b32 v120, 0x7fff, v111\n\t" \
    "v_add_u32 v120, v121, v120\n\t" \
    "ds_read_b128 v[72:75], v120\n\t" \
    "s_add_i32 m0, s56, 1024\n\t" \
    "v_add_u32 v131, v127, v131\n\t" \
    "global_load_lds_dwordx4 v131, s[40:41]\n\t" \
    "s_add_i32 m0, s57, 1024\n\t" \
    "v_add_u32 v135, v128, v135\n\t" \
    "global_load_lds_dwordx4 v135, s[40:41]\n\t" \
    "s_waitcnt vmcnt(14)\n\t" \
    "ds_read_b128 v[100:103], v125 offset:1024\n\t" \
    "ds_read_b128 v[104:107], v126 offset:1024\n\t" \
    "ds_read_b32 v108, v117 offset:1024\n\t" \
    "ds_read_b32 v109, v117 offset:1032\n\t" \
    "v_cvt_pk_f16_f32 v92, v40, v41\n\t" \
    "v_cvt_pk_f16_f32 v93, v42, v43\n\t" \
    "v_cvt_pk_f16_f32 v94, v44, v45\n\t" \
    "v_cvt_pk_f16_f32 v95, v46, v47\n\t" \
    "s_add_i32 m0, s56, 2048\n\t" \
    "v_add_u32 v132, v127, v132\n\t" \
    "global_load_lds_dwordx4 v132, s[40:41]\n\t" \
    "s_waitcnt lgkmcnt(4)\n\t" \
    "v_pk_mul_f16 v76, v92, v76\n\t" \
    "v_pk_mul_f16 v77, v93, v77\n\t" \
    "v_pk_mul_f16 v78, v94, v78\n\t" \
    "v_pk_mul_f16 v79, v95, v79\n\t" \
    "s_add_i32 m0, s57, 2048\n\t" \
    "v_add_u32 v136, v128, v136\n\t" \
    "global_load_lds_dwordx4 v136, s[40:41]\n\t" \
    "v_cvt_pk_f16_f32 v96, v56, v57\n\t" \
    "v_cvt_pk_f16_f32 v97, v58, v59\n\t" \
    "v_mfma_f32_32x32x16_f16 v[0:15], v[88:91], v[76:79], v[0:15]\n\t" \
    "v_cvt_pk_f16_f32 v98, v60, v61\n\t" \
    "v_cvt_pk_f16_f32 v99, v62, v63\n\t" \
    "s_add_i32 m0, s60, 2048\n\t" \
    "v_add_u32 v138, v122, v138\n\t" \
    "global_load_lds_dwordx4 v138, s[40:41]\n\t" \
    "v_pk_mul_f16 v80, v96, v80\n\t" \
    "v_pk_mul_f16 v81, v97, v81\n\t" \
    "v_pk_mul_f16 v82, v98, v82\n\t" \
    "v_pk_mul_f16 v83, v99, v83\n\t" \
    "s_add_i32 m0, s56, 3072\n\t" \
    "v_add_u32 v133, v127, v133\n\t" \
    "global_load_lds_dwordx4 v133, s[40:41]\n\t" \
    "v_cmp_lt_u32 vcc, 0x7fff, v111\n\t" \
    "v_lshrrev_b32 v110, v123, v112\n\t" \
    "s_cbranch_vccnz LS_RARE_0_%=\n\t" \
    "LS_BACK_0_%=:\n\t" \
    "v_mfma_f32_32x32x16_f16 v[16:31], v[88:91], v[80:83], v[16:31]\n\t" \
    "s_add_i32 m0, s60, 3072\n\t" \
    "v_add_u32 v139, v122, v139\n\t" \
    "global_load_lds_dwordx4 v139, s[40:41]\n\t" \
    "v_and_b32 v113, 0xff, v110\n\t" \
    "v_lshlrev_b32 v113, 4, v113\n\t" \
    "ds_read_b128 v[88:91], v113 offset:17408\n\t" \
    "s_add_i32 m0, s57, 3072\n\t" \
    "v_add_u32 v137, v128, v137\n\t" \
    "global_load_lds_dwordx4 v137, s[40:41]\n\t" \
    "s_waitcnt vmcnt(10) lgkmcnt(1)\n\t" \
    "s_barrier\n\t" \
    "s_add_i32 s46, s46, 1\n\t" \
    "s_cmp_eq_u32 s46, s47\n\t" \
    "s_cbranch_scc1 LS_END_%=\n\t" \
    "s_add_i32 m0, s58, 0\n\t" \
    "s_mov_b64 exec, s[52:53]\n\t" \
    "global_load_lds_dwordx4 v124, s[48:49]\n\t" \
    "s_mov_b64 exec, -1\n\t" \
    "s_add_i32 m0, s59, 0\n\t" \
    "s_mov_b64 exec, s[52:53]\n\t" \
    "global_load_lds_dwordx4 v124, s[50:51]\n\t" \
    "s_mov_b64 exec, -1\n\t" \
    "s_add_i32 m0, s56, 4096\n\t" \
    "v_add_u32 v100, v127, v100\n\t" \
    "global_load_lds_dwordx4 v100, s[40:41]\n\t" \
    "ds_read_b64_tr_b16 v[76:77], v114 offset:0\n\t" \
    "ds_read_b64_tr_b16 v[78:79], v114 offset:1024\n\t" \
    "ds_read_b64_tr_b16 v[80:81], v115 offset:0\n\t" \
    "ds_read_b64_tr_b16 v[82:83], v115 offset:1024\n\t" \
    "v_mfma_f32_32x32x16_bf16 v[32:47], v[72:75], v[64:67], 0\n\t" \
    "s_add_i32 m0, s57, 4096\n\t" \
    "v_add_u32 v104, v128, v104\n\t" \
    "global_load_lds_dwordx4 v104, s[40:41]\n\t" \
    "ds_read_u16 v111, v118 offset:1152\n\t" \
    "ds_read_b32 v112, v119 offset:1216\n\t" \
    "v_mfma_f32_32x32x16_bf16 v[48:63], v[72:75], v[68:71], 0\n\t" \
    "s_add_i32 m0, s56, 5120\n\t" \
    "v_add_u32 v101, v127, v101\n\t" \
    "global_load_lds_dwordx4 v101, s[40:41]\n\t" \
    "s_add_i32 m0, s57, 5120\n\t" \
    "v_add_u32 v105, v128, v105\n\t" \
    "global_load_lds_dwordx4 v105, s[40:41]\n\t" \
    "ds_read_b128 v[130:133], v125 offset:1040\n\t" \
    "ds_read_b128 v[134:137], v126 offset:1040\n\t" \
    "ds_read_b32 v138, v117 offset:1040\n\t" \
    "ds_read_b32 v139, v117 offset:1048\n\t" \
    "s_add_i32 s61, s46, 2\n\t" \
    "s_cmp_lt_u32 s61, s54\n\t" \
    "s_cselect_b32 s61, 512, 0\n\t" \
    "s_add_u32 s48, s48, s61\n\t" \
    "s_addc_u32 s49, s49, 0\n\t" \
    "s_add_i32 s61, s46, 2\n\t" \
    "s_cmp_lt_u32 s61, s55\n\t" \
    "s_cselect_b32 s61, 512, 0\n\t" \
    "s_add_u32 s50, s50, s61\n\t" \
    "s_addc_u32 s51, s51, 0\n\t" \
    "v_cvt_pk_f16_f32 v92, v32, v33\n\t" \
    "v_cvt_pk_f16_f32 v93, v34, v35\n\t" \
    "v_cvt_pk_f16_f32 v94, v36, v37\n\t" \
    "v_cvt_pk_f16_f32 v95, v38, v39\n\t" \
    "s_add_i32 m0, s56, 6144\n\t" \
    "v_add_u32 v102, v127, v102\n\t" \
    "global_load_lds_dwordx4 v102, s[40:41]\n\t" \
    "s_waitcnt lgkmcnt(4)\n\t" \
    "v_pk_mul_f16 v76, v92, v76\n\t" \
    "v_pk_mul_f16 v77, v93, v77\n\t" \
    "v_pk_mul_f16 v78, v94, v78\n\t" \
    "v_pk_mul_f16 v79, v95, v79\n\t" \
    "s_add_i32 m0, s57, 6144\n\t" \
    "v_add_u32 v106, v128, v106\n\t" \
    "global_load_lds_dwordx4 v106, s[40:41]\n\t" \
    "v_cvt_pk_f16_f32 v96, v48, v49\n\t" \
    "v_cvt_pk_f16_f32 v97, v50, v51\n\t" \
    "v_mfma_f32_32x32x16_f16 v[0:15], v[88:91], v[76:79], v[0:15]\n\t" \
    "v_cvt_pk_f16_f32 v98, v52, v53\n\t" \
    "v_cvt_pk_f16_f32 v99, v54, v55\n\t" \
    "s_add_i32 m0, s60, 0\n\t" \
    "v_add_u32 v108, v122, v108\n\t" \
    "global_load_lds_dwordx4 v108, s[40:41]\n\t" \
    "v_pk_mul_f16 v80, v96, v80\n\t" \
    "v_pk_mul_f16 v81, v97, v81\n\t" \
    "v_pk_mul_f16 v82, v98, v82\n\t" \
    "v_pk_mul_f16 v83, v99, v83\n\t" \
    "s_add_i32 m0, s56, 7168\n\t" \
    "v_add_u32 v103, v127, v103\n\t" \
    "global_load_lds_dwordx4 v103, s[40:41]\n\t" \
    "v_mfma_f32_32x32x16_f16 v[16:31], v[88:91], v[80:83], v[16:31]\n\t" \
    "s_add_i32 m0, s60, 1024\n\t" \
    "v_add_u32 v109, v122, v109\n\t" \
    "global_load_lds_dwordx4 v109, s[40:41]\n\t" \
    "v_lshrrev_b32 v113, 12, v110\n\t" \
    "v_and_b32 v113, 0xff0, v113\n\t" \
    "ds_read_b128 v[88:91], v113 offset:17408\n\t" \
    "s_add_i32 m0, s57, 7168\n\t" \
    "v_add_u32 v107, v128, v107\n\t" \
    "global_load_lds_dwordx4 v107, s[40:41]\n\t" \
    "s_waitcnt vmcnt(12) lgkmcnt(1)\n\t" \
    "s_barrier\n\t" \
    "s_add_i32 m0, s56, 8192\n\t" \
    "v_add_u32 v130, v127, v130\n\t" \
    "global_load_lds_dwordx4 v130, s[40:41]\n\t" \
    "ds_read_b64_tr_b16 v[76:77], v114 offset:2048\n\t" \
    "ds_read_b64_tr_b16 v[78:79], v114 offset:3072\n\t" \
    "ds_read_b64_tr_b16 v[80:81], v115 offset:2048\n\t" \
    "ds_read_b64_tr_b16 v[82:83], v115 offset:3072\n\t" \
    "s_add_i32 m0, s57, 8192\n\t" \
    "v_add_u32 v134, v128, v134\n\t" \
    "global_load_lds_dwordx4 v134, s[40:41]\n\t" \
    "v_and_b32 v120, 0x7fff, v111\n\t" \
    "v_add_u32 v120, v121, v120\n\t" \
    "ds_read_b128 v[72:75], v120\n\t" \
    "s_add_i32 m0, s56, 9216\n\t" \
    "v_add_u32 v131, v127, v131\n\t" \
    "global_load_lds_dwordx4 v131, s[40:41]\n\t" \
    "s_add_i32 m0, s57, 9216\n\t" \
    "v_add_u32 v135, v128, v135\n\t" \
    "global_load_lds_dwordx4 v135, s[40:41]\n\t" \
    "s_waitcnt vmcnt(14)\n\t" \
    "ds_read_b128 v[100:103], v125 offset:0\n\t" \
    "ds_read_b128 v[104:107], v126 offset:0\n\t" \
    "ds_read_b32 v108, v117 offset:0\n\t" \
    "ds_read_b32 v109, v117 offset:8\n\t" \
    "v_cvt_pk_f16_f32 v92, v40, v41\n\t" \
    "v_cvt_pk_f16_f32 v93, v42, v43\n\t" \
    "v_cvt_pk_f16_f32 v94, v44, v45\n\t" \
    "v_cvt_pk_f16_f32 v95, v46, v47\n\t" \
    "s_add_i32 m0, s56, 10240\n\t" \
    "v_add_u32 v132, v127, v132\n\t" \
    "global_load_lds_dwordx4 v132, s[40:41]\n\t" \
    "s_waitcnt lgkmcnt(4)\n\t" \
    "v_pk_mul_f16 v76, v92, v76\n\t" \
    "v_pk_mul_f16 v77, v93, v77\n\t" \
    "v_pk_mul_f16 v78, v94, v78\n\t" \
    "v_pk_mul_f16 v79, v95, v79\n\t" \
    "s_add_i32 m0, s57, 10240\n\t" \
    "v_add_u32 v136, v128, v136\n\t" \
    "global_load_lds_dwordx4 v136, s[40:41]\n\t" \
    "v_cvt_pk_f16_f32 v96, v56, v57\n\t" \
    "v_cvt_pk_f16_f32 v97, v58, v59\n\t" \
    "v_mfma_f32_32x32x16_f16 v[0:15], v[88:91], v[76:79], v[0:15]\n\t" \
    "v_cvt_pk_f16_f32 v98, v60, v61\n\t" \
    "v_cvt_pk_f16_f32 v99, v62, v63\n\t" \
    "s_add_i32 m0, s60, 2048\n\t" \
    "v_add_u32 v138, v122, v138\n\t" \
    "global_load_lds_dwordx4 v138, s[40:41]\n\t" \
    "v_pk_mul_f16 v80, v96, v80\n\t" \
    "v_pk_mul_f16 v81, v97, v81\n\t" \
    "v_pk_mul_f16 v82, v98, v82\n\t" \
    "v_pk_mul_f16 v83, v99, v83\n\t" \
    "s_add_i32 m0, s56, 11264\n\t" \
    "v_add_u32 v133, v127, v133\n\t" \
    "global_load_lds_dwordx4 v133, s[40:41]\n\t" \
    "v_cmp_lt_u32 vcc, 0x7fff, v111\n\t" \
    "v_lshrrev_b32 v110, v123, v112\n\t" \
    "s_cbranch_vccnz LS_RARE_1_%=\n\t" \
    "LS_BACK_1_%=:\n\t" \
    "v_mfma_f32_32x32x16_f16 v[16:31], v[88:91], v[80:83], v[16:31]\n\t" \
    "s_add_i32 m0, s60, 3072\n\t" \
    "v_add_u32 v139, v122, v139\n\t" \
    "global_load_lds_dwordx4 v139, s[40:41]\n\t" \
    "v_and_b32 v113, 0xff, v110\n\t" \
    "v_lshlrev_b32 v113, 4, v113\n\t" \
    "ds_read_b128 v[88:91], v113 offset:17408\n\t" \
    "s_add_i32 m0, s57, 11264\n\t" \
    "v_add_u32 v137, v128, v137\n\t" \
    "global_load_lds_dwordx4 v137, s[40:41]\n\t" \
    "s_waitcnt vmcnt(10) lgkmcnt(1)\n\t" \
    "s_barrier\n\t" \
    "s_add_i32 s46, s46, 1\n\t" \
    "s_cmp_eq_u32 s46, s47\n\t" \
    "s_cbranch_scc1 LS_END_%=\n\t" \
    "s_add_i32 m0, s58, 512\n\t" \
    "s_mov_b64 exec, s[52:53]\n\t" \
    "global_load_lds_dwordx4 v124, s[48:49]\n\t" \
    "s_mov_b64 exec, -1\n\t" \
    "s_add_i32 m0, s59, 512\n\t" \
    "s_mov_b64 exec, s[52:53]\n\t" \
    "global_load_lds_dwordx4 v124, s[50:51]\n\t" \
    "s_mov_b64 exec, -1\n\t" \
    "s_add_i32 m0, s56, 0\n\t" \
    "v_add_u32 v100, v127, v100\n\t" \
    "global_load_lds_dwordx4 v100, s[40:41]\n\t" \
    "ds_read_b64_tr_b16 v[76:77], v114 offset:0\n\t" \
    "ds_read_b64_tr_b16 v[78:79], v114 offset:1024\n\t" \
    "ds_read_b64_tr_b16 v[80:81], v115 offset:0\n\t" \
    "ds_read_b64_tr_b16 v[82:83], v115 offset:1024\n\t" \
    "v_mfma_f32_32x32x16_bf16 v[32:47], v[72:75], v[64:67], 0\n\t" \
    "s_add_i32 m0, s57, 0\n\t" \
    "v_add_u32 v104, v128, v104\n\t" \
    "global_load_lds_dwordx4 v104, s[40:41]\n\t" \
    "ds_read_u16 v111, v118 offset:128\n\t" \
    "ds_read_b32 v112, v119 offset:192\n\t" \
    "v_mfma_f32_32x32x16_bf16 v[48:63], v[72:75], v[68:71], 0\n\t" \
    "s_add_i32 m0, s56, 1024\n\t" \
    "v_add_u32 v101, v127, v101\n\t" \
    "global_load_lds_dwordx4 v101, s[40:41]\n\t" \
    "s_add_i32 m0, s57, 1024\n\t" \
    "v_add_u32 v105, v128, v105\n\t" \
    "global_load_lds_dwordx4 v105, s[40:41]\n\t" \
    "ds_read_b128 v[130:133], v125 offset:16\n\t" \
    "ds_read_b128 v[134:137], v126 offset:16\n\t" \
    "ds_read_b32 v138, v117 offset:16\n\t" \
    "ds_read_b32 v139, v117 offset:24\n\t" \
    "s_add_i32 s61, s46, 2\n\t" \
    "s_cmp_lt_u32 s61, s54\n\t" \
    "s_cselect_b32 s61, 512, 0\n\t" \
    "s_add_u32 s48, s48, s61\n\t" \
    "s_addc_u32 s49, s49, 0\n\t" \
    "s_add_i32 s61, s46, 2\n\t" \
    "s_cmp_lt_u32 s61, s55\n\t" \
    "s_cselect_b32 s61, 512, 0\n\t" \
    "s_add_u32 s50, s50, s61\n\t" \
    "s_addc_u32 s51, s51, 0\n\t" \
    "v_cvt_pk_f16_f32 v92, v32, v33\n\t" \
    "v_cvt_pk_f16_f32 v93, v34, v35\n\t" \
    "v_cvt_pk_f16_f32 v94, v36, v37\n\t" \
    "v_cvt_pk_f16_f32 v95, v38, v39\n\t" \
    "s_add_i32 m0, s56, 2048\n\t" \
    "v_add_u32 v102, v127, v102\n\t" \
    "global_load_lds_dwordx4 v102, s[40:41]\n\t" \
    "s_waitcnt lgkmcnt(4)\n\t" \
    "v_pk_mul_f16 v76, v92, v76\n\t" \
    "v_pk_mul_f16 v77, v93, v77\n\t" \
    "v_pk_mul_f16 v78, v94, v78\n\t" \
    "v_pk_mul_f16 v79, v95, v79\n\t" \
    "s_add_i32 m0, s57, 2048\n\t" \
    "v_add_u32 v106, v128, v106\n\t" \
    "global_load_lds_dwordx4 v106, s[40:41]\n\t" \
    "v_cvt_pk_f16_f32 v96, v48, v49\n\t" \
    "v_cvt_pk_f16_f32 v97, v50, v51\n\t" \
    "v_mfma_f32_32x32x16_f16 v[0:15], v[88:91], v[76:79], v[0:15]\n\t" \
    "v_cvt_pk_f16_f32 v98, v52, v53\n\t" \
    "v_cvt_pk_f16_f32 v99, v54, v55\n\t" \
    "s_add_i32 m0, s60, 0\n\t" \
    "v_add_u32 v108, v122, v108\n\t" \
    "global_load_lds_dwordx4 v108, s[40:41]\n\t" \
    "v_pk_mul_f16 v80, v96, v80\n\t" \
    "v_pk_mul_f16 v81, v97, v81\n\t" \
    "v_pk_mul_f16 v82, v98, v82\n\t" \
    "v_pk_mul_f16 v83, v99, v83\n\t" \
    "s_add_i32 m0, s56, 3072\n\t" \
    "v_add_u32 v103, v127, v103\n\t" \
    "global_load_lds_dwordx4 v103, s[40:41]\n\t" \
    "v_mfma_f32_32x32x16_f16 v[16:31], v[88:91], v[80:83], v[16:31]\n\t" \
    "s_add_i32 m0, s60, 1024\n\t" \
    "v_add_u32 v109, v122, v109\n\t" \
    "global_load_lds_dwordx4 v109, s[40:41]\n\t" \
    "v_lshrrev_b32 v113, 12, v110\n\t" \
    "v_and_b32 v113, 0xff0, v113\n\t" \
    "ds_read_b128 v[88:91], v113 offset:17408\n\t" \
    "s_add_i32 m0, s57, 3072\n\t" \
    "v_add_u32 v107, v128, v107\n\t" \
    "global_load_lds_dwordx4 v107, s[40:41]\n\t" \
    "s_waitcnt vmcnt(12) lgkmcnt(1)\n\t" \
    "s_barrier\n\t" \
    "s_add_i32 m0, s56, 4096\n\t" \
    "v_add_u32 v130, v127, v130\n\t" \
    "global_load_lds_dwordx4 v130, s[40:41]\n\t" \
    "ds_read_b64_tr_b16 v[76:77], v114 offset:2048\n\t" \
    "ds_read_b64_tr_b16 v[78:79], v114 offset:3072\n\t" \
    "ds_read_b64_tr_b16 v[80:81], v115 offset:2048\n\t" \
    "ds_read_b64_tr_b16 v[82:83], v115 offset:3072\n\t" \
    "s_add_i32 m0, s57, 4096\n\t" \
    "v_add_u32 v134, v128, v134\n\t" \
    "global_load_lds_dwordx4 v134, s[40:41]\n\t" \
    "v_and_b32 v120, 0x7fff, v111\n\t" \
    "v_add_u32 v120, v121, v120\n\t" \
    "ds_read_b128 v[72:75], v120\n\t" \
    "s_add_i32 m0, s56, 5120\n\t" \
    "v_add_u32 v131, v127, v131\n\t" \
    "global_load_lds_dwordx4 v131, s[40:41]\n\t" \
    "s_add_i32 m0, s57, 5120\n\t" \
    "v_add_u32 v135, v128, v135\n\t" \
    "global_load_lds_dwordx4 v135, s[40:41]\n\t" \
    "s_waitcnt vmcnt(14)\n\t" \
    "ds_read_b128 v[100:103], v125 offset:512\n\t" \
    "ds_read_b128 v[104:107], v126 offset:512\n\t" \
    "ds_read_b32 v108, v117 offset:512\n\t" \
    "ds_read_b32 v109, v117 offset:520\n\t" \
    "v_cvt_pk_f16_f32 v92, v40, v41\n\t" \
    "v_cvt_pk_f16_f32 v93, v42, v43\n\t" \
    "v_cvt_pk_f16_f32 v94, v44, v45\n\t" \
    "v_cvt_pk_f16_f32 v95, v46, v47\n\t" \
    "s_add_i32 m0, s56, 6144\n\t" \
    "v_add_u32 v132, v127, v132\n\t" \
    "global_load_lds_dwordx4 v132, s[40:41]\n\t" \
    "s_waitcnt lgkmcnt(4)\n\t" \
    "v_pk_mul_f16 v76, v92, v76\n\t" \
    "v_pk_mul_f16 v77, v93, v77\n\t" \
    "v_pk_mul_f16 v78, v94, v78\n\t" \
    "v_pk_mul_f16 v79, v95, v79\n\t" \
    "s_add_i32 m0, s57, 6144\n\t" \
    "v_add_u32 v136, v128, v136\n\t" \
    "global_load_lds_dwordx4 v136, s[40:41]\n\t" \
    "v_cvt_pk_f16_f32 v96, v56, v57\n\t" \
    "v_cvt_pk_f16_f32 v97, v58, v59\n\t" \
    "v_mfma_f32_32x32x16_f16 v[0:15], v[88:91], v[76:79], v[0:15]\n\t" \
    "v_cvt_pk_f16_f32 v98, v60, v61\n\t" \
    "v_cvt_pk_f16_f32 v99, v62, v63\n\t" \
    "s_add_i32 m0, s60, 2048\n\t" \
    "v_add_u32 v138, v122, v138\n\t" \
    "global_load_lds_dwordx4 v138, s[40:41]\n\t" \
    "v_pk_mul_f16 v80, v96, v80\n\t" \
    "v_pk_mul_f16 v81, v97, v81\n\t" \
    "v_pk_mul_f16 v82, v98, v82\n\t" \
    "v_pk_mul_f16 v83, v99, v83\n\t" \
    "s_add_i32 m0, s56, 7168\n\t" \
    "v_add_u32 v133, v127, v133\n\t" \
    "global_load_lds_dwordx4 v133, s[40:41]\n\t" \
    "v_cmp_lt_u32 vcc, 0x7fff, v111\n\t" \
    "v_lshrrev_b32 v110, v123, v112\n\t" \
    "s_cbranch_vccnz LS_RARE_2_%=\n\t" \
    "LS_BACK_2_%=:\n\t" \
    "v_mfma_f32_32x32x16_f16 v[16:31], v[88:91], v[80:83], v[16:31]\n\t" \
    "s_add_i32 m0, s60, 3072\n\t" \
    "v_add_u32 v139, v122, v139\n\t" \
    "global_load_lds_dwordx4 v139, s[40:41]\n\t" \
    "v_and_b32 v113, 0xff, v110\n\t" \
    "v_lshlrev_b32 v113, 4, v113\n\t" \
    "ds_read_b128 v[88:91], v113 offset:17408\n\t" \
    "s_add_i32 m0, s57, 7168\n\t" \
    "v_add_u32 v137, v128, v137\n\t" \
    "global_load_lds_dwordx4 v137, s[40:41]\n\t" \
    "s_waitcnt vmcnt(10) lgkmcnt(1)\n\t" \
    "s_barrier\n\t" \
    "s_add_i32 s46, s46, 1\n\t" \
    "s_cmp_eq_u32 s46, s47\n\t" \
    "s_cbranch_scc1 LS_END_%=\n\t" \
    "s_branch LS_LOOP_%=\n\t" \
    "LS_RARE_0_%=:\n\t" \
    "v_and_b32 v120, 0x7fff, v111\n\t" \
    "v_lshlrev_b32 v120, 5, v120\n\t" \
    "v_cndmask_b32 v120, 0, v120, vcc\n\t" \
    "v_lshl_add_u32 v120, v123, 1, v120\n\t" \
    "global_load_dwordx4 v[92:95], v120, s[44:45]\n\t" \
    "s_waitcnt vmcnt(0) ; END\n\t" \
    "v_cndmask_b32 v72, v72, v92, vcc\n\t" \
    "v_cndmask_b32 v73, v73, v93, vcc\n\t" \
    "v_cndmask_b32 v74, v74, v94, vcc\n\t" \
    "v_cndmask_b32 v75, v75, v95, vcc\n\t" \
    "s_nop 1\n\t" \
    "s_branch LS_BACK_0_%=\n\t" \
    "LS_RARE_1_%=:\n\t" \
    "v_and_b32 v120, 0x7fff, v111\n\t" \
    "v_lshlrev_b32 v120, 5, v120\n\t" \
    "v_cndmask_b32 v120, 0, v120, vcc\n\t" \
    "v_lshl_add_u32 v120, v123, 1, v120\n\t" \
    "global_load_dwordx4 v[92:95], v120, s[44:45]\n\t" \
    "s_waitcnt vmcnt(0) ; END\n\t" \
    "v_cndmask_b32 v72, v72, v92, vcc\n\t" \
    "v_cndmask_b32 v73, v73, v93, vcc\n\t" \
    "v_cndmask_b32 v74, v74, v94, vcc\n\t" \
    "v_cndmask_b32 v75, v75, v95, vcc\n\t" \
    "s_nop 1\n\t" \
    "s_branch LS_BACK_1_%=\n\t" \
    "LS_RARE_2_%=:\n\t" \
    "v_and_b32 v120, 0x7fff, v111\n\t" \
    "v_lshlrev_b32 v120, 5, v120\n\t" \
    "v_cndmask_b32 v120, 0, v120, vcc\n\t" \
    "v_lshl_add_u32 v120, v123, 1, v120\n\t" \
    "global_load_dwordx4 v[92:95], v120, s[44:45]\n\t" \
    "s_waitcnt vmcnt(0) ; END\n\t" \
    "v_cndmask_b32 v72, v72, v92, vcc\n\t" \
    "v_cndmask_b32 v73, v73, v93, vcc\n\t" \
    "v_cndmask_b32 v74, v74, v94, vcc\n\t" \
    "v_cndmask_b32 v75, v75, v95, vcc\n\t" \
    "s_nop 1\n\t" \
    "s_branch LS_BACK_2_%=\n\t" \
    "LS_END_%=:\n\t" \
    "s_waitcnt vmcnt(0) lgkmcnt(0) ; END\n\t" \
    "s_barrier ; END\n\t" \
    "s_nop 7\n\t" \
    "s_nop 7\n\t"
#define K2_S_CLOBBERS "v32", "v33", "v34", "v35", "v36", "v37", "v38", "v39", "v40", "v41", "v42", "v43", "v44", "v45", "v46", "v47", "v48", "v49", "v50", "v51", "v52", "v53", "v54", "v55", "v56", "v57", "v58", "v59", "v60", "v61", "v62", "v63", "v76", "v77", "v78", "v79", "v80", "v81", "v82", "v83", "v84", "v85", "v86", "v87", "v88", "v89", "v90", "v91", "v92", "v93", "v94", "v95", "v96", "v97", "v98", "v99", "v100", "v101", "v102", "v103", "v104", "v105", "v106", "v107", "v108", "v109", "v111", "v112", "v113", "v120", "v129", "v130", "v131", "v132", "v133", "v134", "v135", "v136", "v137", "v138", "v139", "s61", "vcc", "scc", "memory"
template<int STAMP>
__global__ __launch_bounds__(768,3) void k2_main(K2Args p){
  extern __shared__ __attribute__((aligned(16))) char smem[];
  const int lane = threadIdx.x & 63;
  const int w = __builtin_amdgcn_readfirstlane(threadIdx.x >> 6);
  const int b = blockIdx.x;
  const int hf = (b>>2)&1;
  const int bk = (b>>3)*4 + (b&3);
  const int h = lane>>5, c = lane&31;
  const bool isV = w < 8;
  const int g = isV ? bk*8 + w : bk*8 + 4*hf + (w-8);
  unsigned long long t0=0, t1=0, c0=0, tl=0, tb=0; if (STAMP) { t0 = wall_clock64(); c0 = __builtin_amdgcn_s_memtime(); }
  const char* gimg = p.img + (size_t)g*CAP_T*TILE_B;

  dma16(p.AtabG + (ACODE0-ACODEG0)*32 + w*1024 + lane*16, smem + L_ATAB + w*1024);
  if (w < 5) dma16(p.AtabG + (ACODE0-ACODEG0)*32 + (w+12)*1024 + lane*16, smem + L_ATAB + (w+12)*1024);
  if (isV && lane < 32) { char* rec = smem + L_VREC + w*3*TILE_B; dma16(gimg + lane*16, rec); dma16(gimg + TILE_B + lane*16, rec + TILE_B); }
  for (int i = threadIdx.x; i < 1024; i += 768) { const int e = i>>2, j = i&3;
    *(u32*)(smem + L_LUT + 4*i) = (((e>>(2*j))&1) ? 0x3c00u : 0u) | (((e>>(2*j+1))&1) ? 0x3c000000u : 0u); }
  if (sld(p.slow + 2*bk) | sld(p.slow + 2*bk+1)) { WAITVM(0); WAITLG0(); __syncthreads(); k2_slow(p, smem, bk, hf); return; }
  int T = 1;
#pragma unroll
  for (int j=0;j<8;j++) T = max(T, sld(p.ntiles + bk*8 + j));
  const int dsc = sld(p.misc + 1) + sld(p.misc);
  const int gn = sld(p.gnode + g);
  const int n0 = (g>>2)*p.BS1 + (gn&0xff);
  const int gs = min(gn>>8, p.N - n0);
  u32 dbg0 = 0, dbg1 = 0;

  if (isV) {
    char* ring = smem + L_VRING + w*3*4096;
    char* rec  = smem + L_VREC + w*3*TILE_B;
    u32x4 wvv, wvr;
    asm volatile("global_load_dwordx4 %0, %2, off\n\tglobal_load_dwordx4 %1, %2, off offset:1024" : "=&{v[80:83]}"(wvv), "=&{v[84:87]}"(wvr) : "v"(p.wfrag + (2*hf*64 + lane)*4) : "memory");
    char* rimg = ring + 8192; char* rim2 = smem + L_RES + w*1920;
#pragma unroll
    for (int it=0; it<6; it++){ int ci = it*64+lane; int n = ci/12, rem = ci-12*n; n = min(n, max(gs,1)-1);
      int node = min(n0+n, p.N-1);
      if (it < 5 || lane < 56) dma16((const char*)p.sv + (size_t)node*512 + 128 + (rem>>2)*128 + hf*64 + (rem&3)*16, it < 4 ? rimg + it*1024 : rim2 + (it-4)*1024); }
    WAITVM(8);
    const char* tab = p.X;
    const u32 loff = hf*256 + (((lane&15) ^ ((lane>>4)*4))*16);
    { const u32x4 i0 = *(const u32x4*)(rec + (lane>>4)*32), i1 = *(const u32x4*)(rec + (lane>>4)*32 + 16);
#pragma unroll
      for (int q=0;q<4;q++) dma16(tab + (size_t)(i0[q]+loff), ring + q*1024);
#pragma unroll
      for (int q=0;q<4;q++) dma16(tab + (size_t)(i1[q]+loff), ring + 4096 + q*1024); }
    asm volatile("s_waitcnt vmcnt(8) lgkmcnt(0)" : "+{v[80:83]}"(wvv), "+{v[84:87]}"(wvr) :: "memory");
    if (STAMP) tb = wall_clock64();
    __builtin_amdgcn_s_barrier();
    f32x16 ax, ay, az;
#pragma unroll
    for (int r=0;r<16;r++){ const int n = min((r&3)+8*(r>>2)+4*h, 30); const int o = n*192 + 2*c;
      const u16* q0 = (const u16*)(o < 4096 ? rimg + o : rim2 + (o - 4096)); const u16* q1 = (const u16*)(o+64 < 4096 ? rimg + o+64 : rim2 + (o+64 - 4096)); const u16* q2 = (const u16*)(o+128 < 4096 ? rimg + o+128 : rim2 + (o+128 - 4096));
      ax[r]=ldexpf(asf((u32)q0[0]<<16),dsc); ay[r]=ldexpf(asf((u32)q1[0]<<16),dsc); az[r]=ldexpf(asf((u32)q2[0]<<16),dsc); }
    const u32 sel0 = *(const u16*)(rec + 128 + 2*c);
    u32x4 A = *(const u32x4*)(smem + L_ATAB + (sel0&0x7fff) + 16*h);
    fix_A(A, sel0, p.AtabG);
    u32 M = *(const u32*)(rec + 192 + 4*c) >> (8*h);
    asm volatile("s_waitcnt vmcnt(4) lgkmcnt(0)" ::: "memory");
    if (STAMP) t1 = wall_clock64();
    {
      const u32 rb = ldsaddr(rec);
      const int q = (lane&15)>>2, pp = lane&3, ch = (lane>>4)&1;
      const u32 tb_ = ldsaddr(ring) + (4*h+q)*256 + 32*ch + 8*pp;
      u32x4 bases; bases[0] = tb_ + 64*(0^q); bases[1] = tb_ + 64*(1^q); bases[2] = tb_ + 64*(2^q); bases[3] = tb_ + 64*(3^q);
      const u32 rkb = rb + 16*h, rsb = rb + 2*c, rmb = rb + 4*c, at = ldsaddr(smem) + L_ATAB + 16*h;
      u32 t = 0;
      if (p.prio & 1) { if (w >= 4) __builtin_amdgcn_s_setprio(1); }
      const u32 stag = __builtin_amdgcn_readfirstlane((w >= 4 && !(p.prio & 4)) ? 1u : 0u);
      asm volatile(K2_V_LOOP_TEXT
        : "+{v[0:15]}"(ax), "+{v[16:31]}"(ay), "+{v[32:47]}"(az), "+{v[88:91]}"(A), "+{v136}"(M), "+{s46}"(t), "+{s68}"(dbg0), "+{s69}"(dbg1)
        : "{v[80:83]}"(wvv), "{v[84:87]}"(wvr), "{v[140:143]}"(bases), "{v144}"(rkb), "{v145}"(rsb), "{v146}"(rmb),
          "{v148}"(at), "{v149}"(8*h), "{s[44:45]}"(p.AtabG), "{s47}"(T), "{s40}"(stag)
        : K2_V_CLOBBERS);
    }
    if (STAMP) tl = wall_clock64();
    char* oimg = ring;
    int ln = __lane_id(); PIN(ln);
    const int h2 = ln>>5, c2 = ln&31;
    const rsrc_t orr = mkrsrc((char*)p.out + (size_t)min(n0, p.N)*512, max(gs,0)*512);
#pragma unroll
    for (int r=0;r<16;r++){ int n = (r&3)+8*(r>>2)+4*h2; u16* q = (u16*)(oimg + n*192 + 2*c2);
      q[0]=(u16)pk2(ldexpf(ax[r],-dsc),0.f); q[32]=(u16)pk2(ldexpf(ay[r],-dsc),0.f); q[64]=(u16)pk2(ldexpf(az[r],-dsc),0.f); }
#pragma unroll
    for (int it=0; it<6; it++){ int ci = it*64+ln; int n = ci/12, rem = ci-12*n;
      u32x4 x = *(const u32x4*)(oimg + ci*16);
      st16(orr, n*512 + 128 + (rem>>2)*128 + hf*64 + (rem&3)*16, x); }
  } else {
    const int ws = w-8;
    char* ring = smem + L_SRING + ws*2*2048;
    const int vown = 4*hf + ws;
    const int va = ws, vb = ws+4;
    const int ga = bk*8 + va, gb = bk*8 + vb;
    const int hfa = hf, hfb = hf;
    const int nta = sld(p.ntiles + ga), ntb = sld(p.ntiles + gb);
    char* rec  = smem + L_VREC + vown*3*TILE_B;
    char* rimg = ring;
    u32x4 w0, w1;
    asm volatile("global_load_dwordx4 %0, %2, off\n\tglobal_load_dwordx4 %1, %2, off offset:1024" : "=&{v[64:67]}"(w0), "=&{v[68:71]}"(w1) : "v"(p.wfrag + (4*64 + lane)*4) : "memory");
#pragma unroll
    for (int it=0; it<4; it++){ int ci = it*64+lane; int n = min(ci>>3, max(gs,1)-1); int node = min(n0+n, p.N-1);
      dma16((const char*)p.sv + (size_t)node*512 + (ci&7)*16, rimg + it*1024); }
    asm volatile("s_waitcnt vmcnt(0) lgkmcnt(0)" : "+{v[64:67]}"(w0), "+{v[68:71]}"(w1) :: "memory");
    if (STAMP) tb = wall_clock64();
    __builtin_amdgcn_s_barrier();
    f32x16 a0, a1;
#pragma unroll
    for (int r=0;r<16;r++){ int n = (r&3)+8*(r>>2)+4*h; const u16* q = (const u16*)(rimg + n*128 + 2*c); a0[r]=ldexpf(asf((u32)q[0]<<16),dsc); a1[r]=ldexpf(asf((u32)q[32]<<16),dsc); }
    const u32 sel0 = *(const u16*)(rec + 128 + 2*c);
    u32x4 A = *(const u32x4*)(smem + L_ATAB + (sel0&0x7fff) + 16*h);
    fix_A(A, sel0, p.AtabG);
    u32 M = *(const u32*)(rec + 192 + 4*c) >> (8*h);
    const char* ip = rec + ((lane>>3)&3)*32 + (lane>>5)*4;
    const u32 a0_=*(const u32*)(ip), a1_=*(const u32*)(ip+8), a2_=*(const u32*)(ip+16), a3_=*(const u32*)(ip+24);
    WAITLG0();
    const char* tab = p.X;
    const u32 loff = 512 + (((lane&7) ^ (((lane>>4)&1)*4))*16);
    dma16(tab + (size_t)(a0_+loff), ring); dma16(tab + (size_t)(a1_+loff), ring+1024);
    dma16(tab + (size_t)(a2_+loff), ring+2048); dma16(tab + (size_t)(a3_+loff), ring+3072);
    WAITVM(2);
    if (STAMP) t1 = wall_clock64();
    {
      const u32 rb = ldsaddr(rec);
      const int q = (lane&15)>>2, pp = lane&3, ch = (lane>>4)&1;
      const u32 tb_ = ldsaddr(ring) + (4*h+q)*128 + 32*ch + 8*pp;
      const u32 b0 = tb_ + 64*(0^(q>>1)), b1 = tb_ + 64*(1^(q>>1));
      const u32 rkb = rb + 16*h, rib = ldsaddr(ip), rsb = rb + 2*c, rmb = rb + 4*c, at = ldsaddr(smem) + L_ATAB + 16*h;
      const u32 lbase = ldsaddr(smem);
      const u32 rca = lbase + L_VREC + va*3*TILE_B, rcb = lbase + L_VREC + vb*3*TILE_B;
      const u32 rga = lbase + L_VRING + va*3*4096, rgb = lbase + L_VRING + vb*3*4096;
      const u32 ria = rca + (lane>>4)*32, rib2 = rcb + (lane>>4)*32;
      const u32 vloff = hfa*256 + (((lane&15) ^ ((lane>>4)*4))*16), vloffb = vloff + (hfb-hfa)*256;
      const char* gda = p.img + ((size_t)ga*CAP_T + min(2,nta))*TILE_B;
      const char* gdb = p.img + ((size_t)gb*CAP_T + min(2,ntb))*TILE_B;
      u32 t = 0;
      const u32 sown = __builtin_amdgcn_readfirstlane(ldsaddr(ring));
      if (p.prio & 2) __builtin_amdgcn_s_setprio(2);
      asm volatile(K2_S_LOOP_TEXT
        : "+{v[0:15]}"(a0), "+{v[16:31]}"(a1), "+{v[72:75]}"(A), "+{v110}"(M),
          "+{s[48:49]}"(gda), "+{s[50:51]}"(gdb), "+{s46}"(t), "+{s68}"(dbg0), "+{s69}"(dbg1)
        : "{v[64:67]}"(w0), "{v[68:71]}"(w1), "{v114}"(b0), "{v115}"(b1), "{v116}"(rkb), "{v117}"(rib), "{v118}"(rsb), "{v119}"(rmb),
          "{v121}"(at), "{v122}"(loff), "{v123}"(8*h), "{v124}"(lane*16), "{v125}"(ria), "{v126}"(rib2), "{v127}"(vloff), "{v128}"(vloffb),
          "{s[40:41]}"(tab), "{s[44:45]}"(p.AtabG), "{s47}"(T), "{s[52:53]}"(0xffffffffull), "{s54}"(nta), "{s55}"(ntb),
          "{s56}"(__builtin_amdgcn_readfirstlane(rga)), "{s57}"(__builtin_amdgcn_readfirstlane(rgb)), "{s58}"(__builtin_amdgcn_readfirstlane(rca)), "{s59}"(__builtin_amdgcn_readfirstlane(rcb)), "{s60}"(sown)
        : K2_S_CLOBBERS);
    }
    if (STAMP) tl = wall_clock64();
    char* oimg = ring;
    int ln = __lane_id(); PIN(ln);
    const int h2 = ln>>5, c2 = ln&31;
    const rsrc_t orr = mkrsrc((char*)p.out + (size_t)min(n0, p.N)*512, max(gs,0)*512);
#pragma unroll
    for (int r=0;r<16;r++){ int n = (r&3)+8*(r>>2)+4*h2; u16* q = (u16*)(oimg + n*128 + 2*c2); q[0]=(u16)pk2(ldexpf(a0[r],-dsc),0.f); q[32]=(u16)pk2(ldexpf(a1[r],-dsc),0.f); }
#pragma unroll
    for (int it=0; it<4; it++){ int ci = it*64+ln; int n = ci>>3;
      u32x4 x = *(const u32x4*)(oimg + ci*16);
      st16(orr, n*512 + (ci&7)*16, x); }
  }
  if (STAMP) { asm volatile("s_waitcnt vmcnt(0)" ::: "memory"); unsigned long long t2 = wall_clock64(); if (__lane_id()==0){ unsigned long long* q = p.stamps + (size_t)(b*12+w)*4; q[0]=t0; q[1]=t1; q[2]=t2; q[3]=__builtin_amdgcn_s_memtime()-c0; unsigned long long* q2 = p.stamps + 256*12*4 + (size_t)(b*12+w)*2; q2[0]=tl; q2[1]=tb; unsigned* q3 = (unsigned*)(p.stamps + 256*12*6) + (b*12+w)*4; q3[0]=dbg0; q3[1]=dbg1; q3[2]=T; } }
}

__global__ void fb_phi(const u16* sv, const u16* w1, const u16* w2, float* phi){
  const int n = blockIdx.x, t = threadIdx.x;
  __shared__ float s[64], hh[64];
  if (t < 64) s[t] = bf2f(sv[(size_t)n*256+t]);
  __syncthreads();
  if (t < 64) { float a = 0.f; for (int j=0;j<64;j++) a += s[j]*bf2f(w1[j*64+t]); a = rbf(a); hh[t] = rbf(a/(1.f+expf(-a))); }
  __syncthreads();
  float a = 0.f; for (int j=0;j<64;j++) a += hh[j]*bf2f(w2[j*192+t]);
  phi[(size_t)n*192+t] = rbf(a);
}
__global__ void fb_edge(const u16* sv, const u16* pos, const int* src, const int* dst, const u16* freqs, const u16* wr, const float* phi, float* acc, int E){
  const int e = blockIdx.x*4 + (threadIdx.x>>6), c = threadIdx.x&63;
  if (e >= E) return;
  const int s = src[e], d_ = dst[e];
  float r[3], ss = 0.f;
  for (int k=0;k<3;k++){ r[k] = rbf(bf2f(pos[s*3+k]) - bf2f(pos[d_*3+k])); ss += rbf(r[k]*r[k]); }
  const float d = rbf(sqrtf(rbf(rbf(ss)+1e-8f)));
  if (!(d < 5.0f)) return;
  const float fc = rbf(0.5f*rbf(rbf(cosf(rbf(rbf(3.141592653589793f*d)/5.0f)))+1.0f));
  float w[3] = {0.f,0.f,0.f};
  for (int q=0;q<16;q++){ const float rb_ = rbf(rbf(sinf(rbf(d*bf2f(freqs[q]))))/d); for (int j=0;j<3;j++) w[j] += rb_*bf2f(wr[q*192+j*64+c]); }
  float m[3]; for (int j=0;j<3;j++) m[j] = phi[(size_t)s*192+j*64+c]*rbf(w[j])*fc;
  atomicAdd(&acc[(size_t)d_*256+c], m[0]);
  for (int k=0;k<3;k++) atomicAdd(&acc[(size_t)d_*256+64+k*64+c], m[1]*bf2f(sv[(size_t)s*256+64+k*64+c]) + m[2]*rbf(r[k]/d));
}
__global__ void fb_fin(const u16* sv, const float* acc, u16* out, int n){
  const int i = blockIdx.x*256+threadIdx.x; if (i < n) out[i] = (u16)pk2(bf2f(sv[i]) + acc[i], 0.f);
}

extern "C" void kernel_launch(void* const* d_in, const int* in_sizes, int n_in,
                              void* d_out, int out_size, void* d_ws, size_t ws_size,
                              hipStream_t stream) {
  const u16* sv = (const u16*)d_in[0]; const u16* pos = (const u16*)d_in[1];
  const int* src = (const int*)d_in[2]; const int* dst = (const int*)d_in[3];
  const u16* freqs = (const u16*)d_in[4]; const u16* w1 = (const u16*)d_in[5]; const u16* w2 = (const u16*)d_in[6]; const u16* wr = (const u16*)d_in[7];
  const int N = in_sizes[0]/256, E = in_sizes[2];
  const int BS1 = (N + NBK - 1)/NBK, CH = ((E + NBK - 1)/NBK + 3)/4*4;
  const int k1lds = ((N*6 + 1023) & ~1023) + K1_SCR;
  char* ws = (char*)d_ws; size_t o = 0;
  auto take = [&](size_t n){ char* q = ws + o; o += (n + 255) & ~(size_t)255; return q; };
  if (k1lds <= 163840 && CH <= 4096 && E >= 4 && BS1 >= 2 && BS1 <= 124 && N > 16) {
    PrepArgs a;
    a.sv = sv; a.pos = pos; a.src = src; a.dst = dst; a.freqs = freqs; a.w1 = w1; a.w2 = w2; a.wr = wr;
    a.rec = (u32*)take((size_t)NBK*CH*12 + 64); a.desc = (u32*)take(NBK*NBK*4); a.blkmax = (int*)take(NBK*4); a.Atab = (u16*)take(NACODE*32);
    a.misc = (int*)take(64); a.X = take((size_t)N*XROW); a.img = take((size_t)NBK*4*CAP_T*TILE_B);
    a.ntiles = (int*)take(NBK*16); a.gnode = (int*)take(NBK*16); a.slow = (int*)take(NBK*4); a.wfrag = (u32*)take(6*1024);
    a.stamps = nullptr; a.N = N; a.E = E; a.BS1 = BS1; a.CH = CH;
    a.bsmagic = (u32)((0x100000000ull + BS1 - 1)/BS1); a.wkey = (u32)((16ull<<32)/(unsigned)N);
    K2Args k;
    k.sv = sv; k.wr = wr; k.X = a.X; k.AtabG = (const char*)a.Atab; k.img = a.img; k.ntiles = a.ntiles; k.out = (u16*)d_out;
    k.rec = a.rec; k.desc = a.desc; k.misc = a.misc; k.slow = a.slow; k.gnode = a.gnode; k.wfrag = a.wfrag; k.stamps = nullptr;
    k.N = N; k.BS1 = BS1; k.CH = CH; k.prio = K2_PRIO;
    (void)hipFuncSetAttribute((const void*)k1_partition<0>, hipFuncAttributeMaxDynamicSharedMemorySize, k1lds);
    (void)hipFuncSetAttribute((const void*)k15_sort_table<0>, hipFuncAttributeMaxDynamicSharedMemorySize, K15_LDS);
    (void)hipFuncSetAttribute((const void*)k2_main<0>, hipFuncAttributeMaxDynamicSharedMemorySize, K2_LDS);
    k1_partition<0><<<NBK, 1024, k1lds, stream>>>(a);
    k15_sort_table<0><<<2*NBK, 512, K15_LDS, stream>>>(a);
    k2_main<0><<<NBK, 768, K2_LDS, stream>>>(k);
  } else {
    float* phi = (float*)take((size_t)N*192*4); float* acc = (float*)take((size_t)N*256*4);
    (void)hipMemsetAsync(acc, 0, (size_t)N*256*4, stream);
    fb_phi<<<N, 192, 0, stream>>>(sv, w1, w2, phi);
    fb_edge<<<(E+3)/4, 256, 0, stream>>>(sv, pos, src, dst, freqs, wr, phi, acc, E);
    fb_fin<<<(N*256+255)/256, 256, 0, stream>>>(sv, acc, (u16*)d_out, N*256);
  }
}
```
